```python
import torch

def _softmax(x, axis=-1):
    return torch.softmax(x, dim=axis)

def _gelu(x):
    return torch.nn.functional.gelu(x, approximate='tanh')

def _silu(x):
    return torch.nn.functional.silu(x)

def _sigmoid(x):
    return torch.sigmoid(x)

def _layernorm(x, axis=-1, eps=1e-05):
    m = x.mean(dim=axis, keepdim=True)
    v = x.var(dim=axis, keepdim=True, unbiased=False)
    return (x - m) / torch.sqrt(v + eps)

def _scatter_add(tgt, idx, src, axis=0):
    return tgt.clone().index_add_(axis, idx, src)

def _segment_sum(src, seg, n):
    out = torch.zeros((n,) + src.shape[1:], dtype=src.dtype)
    return out.index_add_(0, seg, src)

def _pad(x, pw):
    flat = tuple((v for pair in reversed(pw) for v in pair))
    return torch.nn.functional.pad(x, flat)
kernel_name = 'adaLN_cond__mha_block_cached__dense_activation_silu__decode'

def setup_inputs():

    _w = lambda a: (lambda t: t.to(torch.bfloat16) if t.is_floating_point() else t)(torch.as_tensor(a))

    def _A_setup():
        B, Sq, D = (8, 1, 640)
        H, Sk, Dh = (32, 4096, 128)
        return {'x': _w((lambda *s: torch.randn(*s, dtype=torch.bfloat16) * 0.1)(B, Sq, D)), 'k_cache': _w((lambda *s: torch.randn(*s, dtype=torch.bfloat16) * 0.8)(B, H, Sk, Dh)), 'v_cache': _w((lambda *s: torch.randn(*s, dtype=torch.bfloat16) * 3.2)(B, H, Sk, Dh)), 'wq': _w((lambda *s: torch.randn(*s, dtype=torch.bfloat16) * 0.8)(D, H * Dh) * D ** (-0.5)), 'wo': _w((lambda *s: torch.randn(*s, dtype=torch.bfloat16) * 3.2)(H * Dh, D) * (H * Dh) ** (-0.5))}
    _A_in = _A_setup()

    def _B_setup():
        B, S, D, F = (8, 1, 640, 1728)
        return {'x': _w((lambda *s: torch.randn(*s, dtype=torch.bfloat16) * 0.1)(B, S, D)), 'w1': _w((lambda *s: torch.randn(*s, dtype=torch.bfloat16) * 3.2)(D, F) * D ** (-0.5)), 'w2': _w((lambda *s: torch.randn(*s, dtype=torch.bfloat16) * 3.2)(F, D) * F ** (-0.5))}
    _B_in = _B_setup()
    return {'A_x': _A_in['x'], 'A_k_cache': _A_in['k_cache'], 'A_v_cache': _A_in['v_cache'], 'A_wq': _A_in['wq'], 'A_wo': _A_in['wo'], 'B_x': _B_in['x'], 'B_w1': _B_in['w1'], 'B_w2': _B_in['w2'], 'c': _w((lambda *s: torch.randn(*s, dtype=torch.bfloat16) * 0.2)(8, 640) * 8.0), 'w_mod': _w((lambda *s: torch.randn(*s, dtype=torch.bfloat16) * 0.2)(640, 6 * 640) * 640 ** (-0.5) * 8.0)}

def reference(A_x, A_k_cache, A_v_cache, A_wq, A_wo, B_x, B_w1, B_w2, c, w_mod):

    def _A_ref(x, k_cache, v_cache, wq, wo):
        B, Sq, D = x.shape
        H, Dh = (32, 128)
        q = (lambda x, *d: x.permute(*d))((x @ wq).reshape(B, Sq, H, Dh), 0, 2, 1, 3)
        scores = torch.einsum('bhqd,bhkd->bhqk', q, k_cache) * Dh ** (-0.5)
        p = _softmax(scores, axis=-1)
        o = torch.einsum('bhqk,bhkd->bhqd', p, v_cache)
        return (lambda x, *d: x.permute(*d))(o, 0, 2, 1, 3).reshape(B, Sq, H * Dh) @ wo

    def _B_ref(x, w1, w2):
        h = torch.einsum('bsd,df->bsf', x, w1)
        h = _silu(h)
        out = torch.einsum('bsf,fd->bsd', h, w2)
        return out
    x = A_x
    mod = (_silu(c) @ w_mod).reshape(c.shape[0], 6, 1, -1)
    sa, ca, ga, sb, cb, gb = (mod[:, 0].clone(), mod[:, 1].clone(), mod[:, 2].clone(), mod[:, 3].clone(), mod[:, 4].clone(), mod[:, 5].clone())
    h = _layernorm(x, axis=-1) * (1 + ca) + sa
    x = x + ga * _A_ref(h, A_k_cache, A_v_cache, A_wq, A_wo)
    h = _layernorm(x, axis=-1) * (1 + cb) + sb
    x = x + gb * _B_ref(h, B_w1, B_w2)
    return x
```

```cpp
#include <hip/hip_runtime.h>
#include <hip/hip_bf16.h>
#include <stdint.h>

typedef unsigned short u16;
typedef unsigned long long u64;
using u32x4 = __attribute__((ext_vector_type(4))) unsigned;
using f32x4 = __attribute__((ext_vector_type(4))) float;
using f32x2 = __attribute__((ext_vector_type(2))) float;
using bf16x2 = __attribute__((ext_vector_type(2))) __bf16;
typedef __attribute__((address_space(1))) u64 gu64;

constexpr int NB = 8, DM = 640, NH = 32, SK = 4096, DH = 128, FF = 1728, NMOD = 6 * DM;
constexpr int NT = 512, NW = 8;
constexpr int NFW = FF / 64;
constexpr size_t WS_EPOCH = 0;
constexpr size_t WS_MOD = 4096;
constexpr size_t WS_P1 = WS_MOD + (size_t)NB * NMOD * 8;
constexpr size_t WS_P2 = WS_P1 + (size_t)NB * NH * DM * 4;
constexpr size_t WS_END = WS_P2 + (size_t)NB * NH * DM * 4;
constexpr size_t WS_DBG = 4u << 20;
constexpr unsigned SENT = 0xFFFFFFFFu;
constexpr unsigned SPIN_CAP = 1u << 22;

#define ROT 0
#define KVAUX 2

__device__ __forceinline__ float bf2f(unsigned v) { return __builtin_bit_cast(float, v << 16); }
__device__ __forceinline__ float bflo(unsigned v) { return __builtin_bit_cast(float, v << 16); }
__device__ __forceinline__ float bfhi(unsigned v) { return __builtin_bit_cast(float, v & 0xffff0000u); }
__device__ __forceinline__ unsigned cvtpk(float lo, float hi) {
  unsigned r; asm("v_cvt_pk_bf16_f32 %0, %1, %2" : "=v"(r) : "v"(lo), "v"(hi)); return r;
}
__device__ __forceinline__ float rbf(float x) { return bflo(cvtpk(x, x)); }
__device__ __forceinline__ float dot2(unsigned k, unsigned q, float acc) {
  return __builtin_amdgcn_fdot2_f32_bf16(__builtin_bit_cast(bf16x2, k), __builtin_bit_cast(bf16x2, q), acc, false);
}
template <int CTRL> __device__ __forceinline__ float dpp(float x) {
  return __builtin_bit_cast(float, __builtin_amdgcn_mov_dpp(__builtin_bit_cast(int, x), CTRL, 0xf, 0xf, true));
}
__device__ __forceinline__ float row_sum(float x) {
  x += dpp<0x128>(x); x += dpp<0x124>(x); x += dpp<0x122>(x); x += dpp<0x121>(x); return x;
}
__device__ __forceinline__ float fold32(float a, float b) {
  auto s = __builtin_amdgcn_permlane32_swap(__float_as_uint(a), __float_as_uint(b), false, false);
  return __uint_as_float(s[0]) + __uint_as_float(s[1]);
}
__device__ __forceinline__ float fold16(float a, float b) {
  auto s = __builtin_amdgcn_permlane16_swap(__float_as_uint(a), __float_as_uint(b), false, false);
  return __uint_as_float(s[0]) + __uint_as_float(s[1]);
}
__device__ __forceinline__ float xrow_sum(float x) { x = fold16(x, x); return fold32(x, x); }
__device__ __forceinline__ float xrow_max(float x) {
  auto s = __builtin_amdgcn_permlane16_swap(__float_as_uint(x), __float_as_uint(x), false, false);
  x = fmaxf(__uint_as_float(s[0]), __uint_as_float(s[1]));
  auto t = __builtin_amdgcn_permlane32_swap(__float_as_uint(x), __float_as_uint(x), false, false);
  return fmaxf(__uint_as_float(t[0]), __uint_as_float(t[1]));
}
__device__ __forceinline__ float wave_sum(float x) { return xrow_sum(row_sum(x)); }
typedef __amdgpu_buffer_rsrc_t rsrc_t;
__device__ __forceinline__ rsrc_t mkrs(const void* p, unsigned bytes) { return __builtin_amdgcn_make_buffer_rsrc((void*)p, 0, (int)bytes, 0x00020000); }
#define BLD(rs_, vo_, so_, aux_) __builtin_amdgcn_raw_buffer_load_b128(rs_, vo_, so_, aux_)
__device__ __forceinline__ u64 ldg(const u64* p) { return __hip_atomic_load((gu64*)p, __ATOMIC_RELAXED, __HIP_MEMORY_SCOPE_AGENT); }
__device__ __forceinline__ void stg(u64* p, u64 v) { __hip_atomic_store((gu64*)p, v, __ATOMIC_RELAXED, __HIP_MEMORY_SCOPE_AGENT); }
__device__ __forceinline__ float silu(float x) { return x / (1.f + __expf(-x)); }
#define CBAR() asm volatile("" ::: "memory")
#define SB() __builtin_amdgcn_sched_barrier(0)
__device__ __forceinline__ void fma8(float* a, float s, u32x4 w) {
#pragma unroll
  for (int k = 0; k < 4; ++k) { a[2 * k] = fmaf(s, bflo(w[k]), a[2 * k]); a[2 * k + 1] = fmaf(s, bfhi(w[k]), a[2 * k + 1]); }
}
__device__ __forceinline__ void fold8(const float* a, float& o0, float& o1) {
  const float u0 = fold32(a[0], a[4]), u1 = fold32(a[1], a[5]), u2 = fold32(a[2], a[6]), u3 = fold32(a[3], a[7]);
  o0 = fold16(u0, u2); o1 = fold16(u1, u3);
}
__device__ __forceinline__ unsigned nosent(float v) { const unsigned u = __float_as_uint(v); return u == SENT ? 0x7FC00000u : u; }

#define TS(i) do {} while (0)

__global__ void __launch_bounds__(NT, 2)
fused_block(const u16* __restrict__ X, const u16* __restrict__ Kc, const u16* __restrict__ Vc, const u16* __restrict__ Wq,
            const u16* __restrict__ Wo, const u16* __restrict__ W1, const u16* __restrict__ W2, const u16* __restrict__ Cc,
            const u16* __restrict__ Wm, u16* __restrict__ Out, unsigned char* ws) {
  __shared__ __attribute__((aligned(16))) float red[NW * DM];
  __shared__ __attribute__((aligned(16))) float vec[DM];
  __shared__ __attribute__((aligned(16))) float modr[4 * DM];
  __shared__ __attribute__((aligned(16))) float mrg[NW * (DH + 4)];
  __shared__ __attribute__((aligned(16))) float ovec[DH];
  const int tid = threadIdx.x, lane = tid & 63, w = __builtin_amdgcn_readfirstlane(tid >> 6), r = lane >> 4, c = lane & 15;
  const int b = blockIdx.x >> 5, h = blockIdx.x & 31;
  const int sl = (h & 7) * 4 + (h >> 3);
  TS(0);
  u64* modg = reinterpret_cast<u64*>(ws + WS_MOD) + (size_t)b * NMOD;
  const __amdgpu_buffer_rsrc_t rs = __builtin_amdgcn_make_buffer_rsrc(ws, 0, (int)WS_END, 0x00020000);
  const unsigned p1off = (unsigned)WS_P1 + (unsigned)b * NH * DM * 4, p2off = (unsigned)WS_P2 + (unsigned)b * NH * DM * 4;
  if (w == 0) {
    const u32x4 sv = {SENT, SENT, SENT, SENT};
#pragma unroll
    for (int j = 0; j < 3; ++j) if (lane + 64 * j < DM / 4) {
      __builtin_amdgcn_raw_buffer_store_b128(sv, rs, p1off + h * DM * 4 + (lane + 64 * j) * 16, 0, 16);
      __builtin_amdgcn_raw_buffer_store_b128(sv, rs, p2off + h * DM * 4 + (lane + 64 * j) * 16, 0, 16);
    }
  }
  CBAR();
  const u64 epoch = ldg(reinterpret_cast<const u64*>(ws + WS_EPOCH + b * 256));
  const int col1 = w < 5 ? 40 * sl + 8 * w : 2 * DM + 80 * sl + 8 * (w - 5);
  const int col2 = 2 * DM + 80 * sl + 8 * (w + 3);
  u32x4 wm[10]; unsigned cv[10], xv[10];
  const rsrc_t rWm = mkrs(Wm, DM * NMOD * 2);
#pragma unroll
  for (int i = 0; i < 10; ++i) wm[i] = BLD(rWm, lane * (NMOD * 2), col1 * 2 + i * 64 * NMOD * 2, 0);
#pragma unroll
  for (int i = 0; i < 10; ++i) { cv[i] = Cc[b * DM + lane + 64 * i]; xv[i] = X[b * DM + lane + 64 * i]; }
  const unsigned xa = X[b * DM + tid], xb = X[b * DM + 512 + (tid & 127)];
  CBAR();
  u32x4 wr[20];
  const int rg = w * 4 + r;
  {
    const rsrc_t rWq = mkrs(Wq + h * DH, (DM * NH * DH - h * DH) * 2);
#pragma unroll
    for (int j = 0; j < 20; ++j) wr[j] = BLD(rWq, rg * 20 * (NH * DH * 2) + c * 16, j * (NH * DH * 2), 0);
  }
  CBAR();
  u32x4 Kr[3][4], Vr[3][4];
  const rsrc_t rK = mkrs(Kc + (size_t)blockIdx.x * SK * DH, SK * DH * 2), rV = mkrs(Vc + (size_t)blockIdx.x * SK * DH, SK * DH * 2);
  const int kvo = lane * 16;
  const int rot = ROT ? (blockIdx.x * ROT) & 31 : 0;
#define ISSUE(s_, i_) do { const int ti_ = ((((i_) + rot) & 31) * 8 + w) * (16 * DH * 2); \
    _Pragma("unroll") for (int t_ = 0; t_ < 4; ++t_) { Kr[s_][t_] = BLD(rK, kvo, ti_ + t_ * 1024, KVAUX); Vr[s_][t_] = BLD(rV, kvo, ti_ + t_ * 1024, KVAUX); } } while (0)
  ISSUE(0, 0); ISSUE(1, 1);
  CBAR();
  const u64 tag = ((epoch + 1) & 0xFFFFFFFFFFFFull) << 16;
  float sc[10];
#pragma unroll
  for (int i = 0; i < 10; ++i) sc[i] = rbf(silu(bf2f(cv[i])));
  {
    float a[8] = {0.f, 0.f, 0.f, 0.f, 0.f, 0.f, 0.f, 0.f};
#pragma unroll
    for (int i = 0; i < 10; ++i) fma8(a, sc[i], wm[i]);
    float o0, o1; fold8(a, o0, o1); o0 = row_sum(o0); o1 = row_sum(o1);
    if (c < 2) stg(modg + col1 + 2 * r + c, tag | (u64)(cvtpk(c ? o1 : o0, 0.f) & 0xffffu));
  }
  CBAR();
  TS(1);
  float mean = 0.f;
#pragma unroll
  for (int i = 0; i < 10; ++i) mean += bf2f(xv[i]);
  mean = wave_sum(mean) * (1.f / DM);
  float var = 0.f;
#pragma unroll
  for (int i = 0; i < 10; ++i) { const float d = bf2f(xv[i]) - mean; var = fmaf(d, d, var); }
  var = wave_sum(var) * (1.f / DM);
  const float rstd = 1.f / sqrtf(var + 1e-5f);
  CBAR();
  u64 gs0 = ldg(modg + tid), gc0 = ldg(modg + DM + tid), gs1 = ldg(modg + 512 + (tid & 127)), gc1 = ldg(modg + DM + 512 + (tid & 127));
  CBAR();
  ISSUE(2, 2);
  CBAR();
  for (unsigned spins = 0;; ++spins) {
    const bool ok = ((gs0 ^ tag) >> 16) == 0 && ((gc0 ^ tag) >> 16) == 0 && ((gs1 ^ tag) >> 16) == 0 && ((gc1 ^ tag) >> 16) == 0;
    if (__all(ok) || spins > SPIN_CAP) break;
    __builtin_amdgcn_s_sleep(2);
    gs0 = ldg(modg + tid); gc0 = ldg(modg + DM + tid); gs1 = ldg(modg + 512 + (tid & 127)); gc1 = ldg(modg + DM + 512 + (tid & 127));
  }
  vec[tid] = (bf2f(xa) - mean) * rstd * (1.f + bf2f((unsigned)gc0 & 0xffffu)) + bf2f((unsigned)gs0 & 0xffffu);
  if (tid < 128) vec[512 + tid] = (bf2f(xb) - mean) * rstd * (1.f + bf2f((unsigned)gc1 & 0xffffu)) + bf2f((unsigned)gs1 & 0xffffu);
  TS(2);
  __builtin_amdgcn_s_waitcnt(0xC07F);
  __builtin_amdgcn_s_barrier();
  CBAR();
  unsigned q[4];
  {
    float a[8] = {0.f, 0.f, 0.f, 0.f, 0.f, 0.f, 0.f, 0.f};
#pragma unroll
    for (int j4 = 0; j4 < 5; ++j4) {
      const f32x4 hv = *reinterpret_cast<const f32x4*>(vec + rg * 20 + j4 * 4);
#pragma unroll
      for (int e = 0; e < 4; ++e) fma8(a, hv[e], wr[j4 * 4 + e]);
    }
    float o0, o1; fold8(a, o0, o1);
    *reinterpret_cast<f32x2*>(red + w * DH + 8 * c + 2 * r) = f32x2{o0, o1};
    __builtin_amdgcn_s_waitcnt(0xC07F);
    __builtin_amdgcn_s_barrier();
    CBAR();
    float s0 = 0.f, s1 = 0.f;
#pragma unroll
    for (int u = 0; u < NW; ++u) { const f32x2 t = *reinterpret_cast<const f32x2*>(red + u * DH + 8 * c + 2 * r); s0 += t[0]; s1 += t[1]; }
    const unsigned pk = cvtpk(s0, s1);
    auto ab = __builtin_amdgcn_permlane16_swap(pk, pk, false, false);
    auto e0 = __builtin_amdgcn_permlane32_swap(ab[0], ab[0], false, false);
    auto e1 = __builtin_amdgcn_permlane32_swap(ab[1], ab[1], false, false);
    q[0] = e0[0]; q[2] = e0[1]; q[1] = e1[0]; q[3] = e1[1];
  }
  TS(3);
#pragma unroll
  for (int i = 0; i < 10; ++i) wm[i] = BLD(rWm, lane * (NMOD * 2), (w < 7 ? col2 : col1) * 2 + i * 64 * NMOD * 2, 0);
  CBAR();
  constexpr float C2 = 0.088388347648318440f * 1.4426950408889634f;
  float m = -3.0e38f, l = 0.f, acc[8] = {0.f, 0.f, 0.f, 0.f, 0.f, 0.f, 0.f, 0.f};
#define PROC(s_) do { float s4_[4]; \
    _Pragma("unroll") for (int t_ = 0; t_ < 4; ++t_) { float d_ = 0.f; \
      _Pragma("unroll") for (int k_ = 0; k_ < 4; ++k_) d_ = dot2(Kr[s_][t_][k_], q[k_], d_); \
      s4_[t_] = row_sum(d_) * C2; } \
    const float mn_ = fmaxf(fmaxf(fmaxf(s4_[0], s4_[1]), fmaxf(s4_[2], s4_[3])), m); \
    const float al_ = __builtin_amdgcn_exp2f(m - mn_); m = mn_; \
    _Pragma("unroll") for (int t_ = 0; t_ < 4; ++t_) s4_[t_] = __builtin_amdgcn_exp2f(s4_[t_] - mn_); \
    l = fmaf(l, al_, (s4_[0] + s4_[1]) + (s4_[2] + s4_[3])); \
    _Pragma("unroll") for (int k_ = 0; k_ < 8; ++k_) acc[k_] *= al_; \
    _Pragma("unroll") for (int t_ = 0; t_ < 4; ++t_) fma8(acc, s4_[t_], Vr[s_][t_]); \
    asm volatile("" : "+v"(acc[0]), "+v"(acc[1]), "+v"(acc[2]), "+v"(acc[3]), "+v"(acc[4]), "+v"(acc[5]), "+v"(acc[6]), "+v"(acc[7]), "+v"(l), "+v"(m) :: "memory"); } while (0)
  PROC(0); SB(); ISSUE(0, 3); SB();
  PROC(1); SB(); ISSUE(1, 4); SB();
  PROC(2); SB(); ISSUE(2, 5); SB();
  CBAR();
  {
    float a[8] = {0.f, 0.f, 0.f, 0.f, 0.f, 0.f, 0.f, 0.f};
#pragma unroll
    for (int i = 0; i < 10; ++i) fma8(a, sc[i], wm[i]);
    float o0, o1; fold8(a, o0, o1); o0 = row_sum(o0); o1 = row_sum(o1);
    if (c < 2 && w < 7) stg(modg + col2 + 2 * r + c, tag | (u64)(cvtpk(c ? o1 : o0, 0.f) & 0xffffu));
  }
  CBAR();
  const int rowo = r * (DM * 2) + c * 16;
  {
    const rsrc_t rWo = mkrs(Wo + (size_t)(h * DH + 16 * w) * DM, 16 * DM * 2);
#pragma unroll
    for (int i = 0; i < 4; ++i)
#pragma unroll
      for (int jb = 0; jb < 5; ++jb) wr[i * 5 + jb] = BLD(rWo, rowo, i * 4 * DM * 2 + jb * 256, 0);
  }
  CBAR();
  for (int it = 6; it < 30; it += 3) {
    PROC(0); SB(); ISSUE(0, it); SB();
    PROC(1); SB(); ISSUE(1, it + 1); SB();
    PROC(2); SB(); ISSUE(2, it + 2); SB();
  }
  PROC(0); SB(); ISSUE(0, 30); SB();
  PROC(1); SB(); ISSUE(1, 31); SB();
  CBAR();
  u64 gm[5];
#pragma unroll
  for (int j = 0; j < 5; ++j) gm[j] = ldg(modg + 2 * DM + tid + NT * j);
  CBAR();
  u32x4 w1r[10], w2r[10];
  const int fs = h < NFW ? h : 0;
  PROC(2); SB();
  CBAR();
  {
    const rsrc_t rW1 = mkrs(W1, DM * FF * 2);
#pragma unroll
    for (int i = 0; i < 10; ++i) w1r[i] = BLD(rW1, lane * (FF * 2), (fs * 64 + 8 * w) * 2 + i * 64 * FF * 2, 0);
  }
  CBAR();
  PROC(0); SB();
  CBAR();
  {
    const rsrc_t rW2 = mkrs(W2 + (size_t)(fs * 64 + 8 * w) * DM, 8 * DM * 2);
#pragma unroll
    for (int i = 0; i < 2; ++i)
#pragma unroll
      for (int jb = 0; jb < 5; ++jb) w2r[i * 5 + jb] = BLD(rW2, rowo, i * 4 * DM * 2 + jb * 256, 0);
  }
  CBAR();
  PROC(1); SB();
  TS(4);
  {
    const float M = xrow_max(m), f = __builtin_amdgcn_exp2f(m - M);
    l = xrow_sum(l * f);
#pragma unroll
    for (int k = 0; k < 8; ++k) acc[k] *= f;
    float o0, o1; fold8(acc, o0, o1);
    *reinterpret_cast<f32x2*>(mrg + w * (DH + 4) + 4 + 8 * c + 2 * r) = f32x2{o0, o1};
    if (lane == 0) { mrg[w * (DH + 4)] = M; mrg[w * (DH + 4) + 1] = l; }
  }
  for (unsigned spins = 0;; ++spins) {
    bool ok = true;
#pragma unroll
    for (int j = 0; j < 5; ++j) ok &= ((gm[j] ^ tag) >> 16) == 0;
    if (__all(ok) || spins > SPIN_CAP) break;
    __builtin_amdgcn_s_sleep(2);
#pragma unroll
    for (int j = 0; j < 5; ++j) gm[j] = ldg(modg + 2 * DM + tid + NT * j);
  }
#pragma unroll
  for (int j = 0; j < 5; ++j) modr[tid + NT * j] = bf2f((unsigned)gm[j] & 0xffffu);
  __builtin_amdgcn_s_waitcnt(0xC07F);
  __builtin_amdgcn_s_barrier();
  CBAR();
  float ov[4];
  {
    float M = -3.0e38f;
#pragma unroll
    for (int u = 0; u < NW; ++u) M = fmaxf(M, mrg[u * (DH + 4)]);
    float L = 0.f; ov[0] = ov[1] = ov[2] = ov[3] = 0.f;
#pragma unroll
    for (int u = 0; u < NW; ++u) {
      const float f = __builtin_amdgcn_exp2f(mrg[u * (DH + 4)] - M);
      L = fmaf(f, mrg[u * (DH + 4) + 1], L);
#pragma unroll
      for (int i = 0; i < 4; ++i) ov[i] = fmaf(f, mrg[u * (DH + 4) + 4 + 16 * w + 4 * i + r], ov[i]);
    }
    const float inv = 1.f / L;
#pragma unroll
    for (int i = 0; i < 4; ++i) ov[i] = rbf(ov[i] * inv);
  }
#pragma unroll
  for (int jb = 0; jb < 5; ++jb) {
    float a[8] = {0.f, 0.f, 0.f, 0.f, 0.f, 0.f, 0.f, 0.f};
#pragma unroll
    for (int i = 0; i < 4; ++i) fma8(a, ov[i], wr[i * 5 + jb]);
    float o0, o1; fold8(a, o0, o1);
    *reinterpret_cast<f32x2*>(red + w * DM + jb * 128 + 8 * c + 2 * r) = f32x2{o0, o1};
  }
  __builtin_amdgcn_s_waitcnt(0xC07F);
  __builtin_amdgcn_s_barrier();
  CBAR();
  if (tid < DM / 4) {
    f32x4 s = *reinterpret_cast<const f32x4*>(red + tid * 4);
#pragma unroll
    for (int u = 1; u < NW; ++u) s += *reinterpret_cast<const f32x4*>(red + u * DM + tid * 4);
    const u32x4 o = {nosent(s[0]), nosent(s[1]), nosent(s[2]), nosent(s[3])};
    __builtin_amdgcn_raw_buffer_store_b128(o, rs, p1off + h * DM * 4 + tid * 16, 0, 16);
  }
  CBAR();
  TS(5);
  {
    f32x4 s[3];
    const int c2 = lane + 128 < DM / 4 ? lane + 128 : lane;
    for (unsigned spins = 0;; ++spins) {
      u32x4 g[12];
#pragma unroll
      for (int p = 0; p < 4; ++p) {
        const unsigned base = p1off + (4 * w + p) * DM * 4;
        g[p * 3 + 0] = __builtin_amdgcn_raw_buffer_load_b128(rs, base + lane * 16, 0, 16);
        g[p * 3 + 1] = __builtin_amdgcn_raw_buffer_load_b128(rs, base + (lane + 64) * 16, 0, 16);
        g[p * 3 + 2] = __builtin_amdgcn_raw_buffer_load_b128(rs, base + c2 * 16, 0, 16);
      }
      bool ok = true;
#pragma unroll
      for (int k = 0; k < 12; ++k) ok &= g[k][0] != SENT && g[k][1] != SENT && g[k][2] != SENT && g[k][3] != SENT;
#pragma unroll
      for (int k = 0; k < 3; ++k)
        s[k] = (__builtin_bit_cast(f32x4, g[k]) + __builtin_bit_cast(f32x4, g[3 + k])) + (__builtin_bit_cast(f32x4, g[6 + k]) + __builtin_bit_cast(f32x4, g[9 + k]));
      if (__all(ok) || spins > SPIN_CAP) break;
      __builtin_amdgcn_s_sleep(4);
      CBAR();
    }
    __builtin_amdgcn_s_barrier();
    CBAR();
    *reinterpret_cast<f32x4*>(red + w * DM + lane * 4) = s[0];
    *reinterpret_cast<f32x4*>(red + w * DM + (lane + 64) * 4) = s[1];
    if (lane < 32) *reinterpret_cast<f32x4*>(red + w * DM + (lane + 128) * 4) = s[2];
  }
  TS(6);
  __builtin_amdgcn_s_waitcnt(0xC07F);
  __builtin_amdgcn_s_barrier();
  CBAR();
  {
    float s = 0.f, s2 = 0.f;
#pragma unroll
    for (int u = 0; u < NW; ++u) { s += red[u * DM + tid]; s2 += red[u * DM + 512 + (tid & 127)]; }
    vec[tid] = rbf(bf2f(xa) + rbf(modr[tid] * rbf(s)));
    if (tid < 128) vec[512 + tid] = rbf(bf2f(xb) + rbf(modr[512 + tid] * rbf(s2)));
  }
  __builtin_amdgcn_s_waitcnt(0xC07F);
  __builtin_amdgcn_s_barrier();
  CBAR();
  if (h < NFW) {
    float x1[10];
    float mu = 0.f;
#pragma unroll
    for (int i = 0; i < 10; ++i) { x1[i] = vec[lane + 64 * i]; mu += x1[i]; }
    mu = wave_sum(mu) * (1.f / DM);
    float vr = 0.f;
#pragma unroll
    for (int i = 0; i < 10; ++i) { const float d = x1[i] - mu; vr = fmaf(d, d, vr); }
    vr = wave_sum(vr) * (1.f / DM);
    const float rs2 = 1.f / sqrtf(vr + 1e-5f);
    float a[8] = {0.f, 0.f, 0.f, 0.f, 0.f, 0.f, 0.f, 0.f};
#pragma unroll
    for (int i = 0; i < 10; ++i) {
      const float h2 = (x1[i] - mu) * rs2 * (1.f + modr[2 * DM + lane + 64 * i]) + modr[DM + lane + 64 * i];
      fma8(a, h2, w1r[i]);
    }
    float o0, o1; fold8(a, o0, o1); o0 = silu(rbf(row_sum(o0))); o1 = silu(rbf(row_sum(o1)));
    const unsigned pk = cvtpk(o0, o1);
    auto ab = __builtin_amdgcn_permlane16_swap(pk, pk, false, false);
    auto e0 = __builtin_amdgcn_permlane32_swap(ab[0], ab[0], false, false);
    auto e1 = __builtin_amdgcn_permlane32_swap(ab[1], ab[1], false, false);
    const unsigned lo = (r & 2) ? e1[0] : e0[0], hi = (r & 2) ? e1[1] : e0[1];
    const float hv0 = (r & 1) ? bfhi(lo) : bflo(lo), hv1 = (r & 1) ? bfhi(hi) : bflo(hi);
#pragma unroll
    for (int jb = 0; jb < 5; ++jb) {
      float a2[8] = {0.f, 0.f, 0.f, 0.f, 0.f, 0.f, 0.f, 0.f};
      fma8(a2, hv0, w2r[jb]); fma8(a2, hv1, w2r[5 + jb]);
      float p0, p1; fold8(a2, p0, p1);
      *reinterpret_cast<f32x2*>(red + w * DM + jb * 128 + 8 * c + 2 * r) = f32x2{p0, p1};
    }
    __builtin_amdgcn_s_waitcnt(0xC07F);
    __builtin_amdgcn_s_barrier();
    CBAR();
    if (tid < DM / 4) {
      f32x4 s = *reinterpret_cast<const f32x4*>(red + tid * 4);
#pragma unroll
      for (int u = 1; u < NW; ++u) s += *reinterpret_cast<const f32x4*>(red + u * DM + tid * 4);
      const u32x4 o = {nosent(s[0]), nosent(s[1]), nosent(s[2]), nosent(s[3])};
      __builtin_amdgcn_raw_buffer_store_b128(o, rs, p2off + h * DM * 4 + tid * 16, 0, 16);
    }
    CBAR();
  }
  TS(7);
  if (w == 3) {
    const int ch = lane % 5, pg = lane / 5;
    f32x4 s;
    for (unsigned spins = 0;; ++spins) {
      u32x4 g[3];
      bool ok = true;
      s = f32x4{0.f, 0.f, 0.f, 0.f};
#pragma unroll
      for (int k = 0; k < 3; ++k) {
        const int p = pg + 12 * k; const bool live = lane < 60 && p < NFW;
        g[k] = __builtin_amdgcn_raw_buffer_load_b128(rs, p2off + (live ? p : 0) * DM * 4 + (h * 5 + ch) * 16, 0, 16);
      }
#pragma unroll
      for (int k = 0; k < 3; ++k) {
        const int p = pg + 12 * k; const bool live = lane < 60 && p < NFW;
        ok &= g[k][0] != SENT && g[k][1] != SENT && g[k][2] != SENT && g[k][3] != SENT;
        if (live) s += __builtin_bit_cast(f32x4, g[k]);
      }
      if (__all(ok) || spins > SPIN_CAP) break;
      __builtin_amdgcn_s_sleep(2);
      CBAR();
    }
    *reinterpret_cast<f32x4*>(mrg + lane * 4) = s;
    __builtin_amdgcn_s_waitcnt(0xC07F);
    CBAR();
    if (lane < 20) {
      float t = 0.f;
#pragma unroll
      for (int u = 0; u < 12; ++u) t += mrg[u * 20 + lane];
      const int col = h * 20 + lane;
      const float o = vec[col] + rbf(modr[3 * DM + col] * rbf(t));
      Out[b * DM + col] = (u16)(cvtpk(o, 0.f) & 0xffffu);
    }
    if (h == 0 && lane == 0) stg(reinterpret_cast<u64*>(ws + WS_EPOCH + b * 256), epoch + 1);
  }
}

extern "C" void kernel_launch(void* const* d_in, const int* in_sizes, int n_in, void* d_out, int out_size, void* d_ws, size_t ws_size,
                              hipStream_t stream) {
  (void)in_sizes; (void)n_in; (void)out_size; (void)ws_size;
  hipLaunchKernelGGL(fused_block, dim3(NB * NH), dim3(NT), 0, stream, (const u16*)d_in[0], (const u16*)d_in[1], (const u16*)d_in[2],
                     (const u16*)d_in[3], (const u16*)d_in[4], (const u16*)d_in[6], (const u16*)d_in[7], (const u16*)d_in[8],
                     (const u16*)d_in[9], (u16*)d_out, (unsigned char*)d_ws);
}
```

```cpp
#include <hip/hip_runtime.h>
#include <hip/hip_bf16.h>
#include <stdint.h>

typedef unsigned short u16;
typedef unsigned long long u64;
using u32x4 = __attribute__((ext_vector_type(4))) unsigned;
using u32x2 = __attribute__((ext_vector_type(2))) unsigned;
using f32x4 = __attribute__((ext_vector_type(4))) float;
using f32x2 = __attribute__((ext_vector_type(2))) float;
using bf16x2 = __attribute__((ext_vector_type(2))) __bf16;
typedef __attribute__((address_space(1))) u64 gu64;

constexpr int NB = 8, DM = 640, NH = 32, SK = 4096, DH = 128, FF = 1728, NMOD = 6 * DM;
constexpr int NT = 512, NW = 8;
constexpr int NFW = FF / 64;
constexpr size_t WS_EPOCH = 0;
constexpr size_t WS_MOD = 4096;
constexpr size_t WS_P1 = WS_MOD + (size_t)NB * NMOD * 8;
constexpr size_t WS_P2 = WS_P1 + (size_t)NB * NH * DM * 4;
constexpr size_t WS_END = WS_P2 + (size_t)NB * NH * DM * 4;
constexpr size_t WS_DBG = 4u << 20;
constexpr unsigned SENT = 0xFFFFFFFFu;
constexpr unsigned SPIN_CAP = 1u << 22;

#define KVAUX 2
#define PACE 1

__device__ __forceinline__ float bf2f(unsigned v) { return __builtin_bit_cast(float, v << 16); }
__device__ __forceinline__ float bflo(unsigned v) { return __builtin_bit_cast(float, v << 16); }
__device__ __forceinline__ float bfhi(unsigned v) { return __builtin_bit_cast(float, v & 0xffff0000u); }
__device__ __forceinline__ unsigned cvtpk(float lo, float hi) {
  unsigned r; asm("v_cvt_pk_bf16_f32 %0, %1, %2" : "=v"(r) : "v"(lo), "v"(hi)); return r;
}
__device__ __forceinline__ float rbf(float x) { return bflo(cvtpk(x, x)); }
__device__ __forceinline__ float dot2(unsigned k, unsigned q, float acc) {
  return __builtin_amdgcn_fdot2_f32_bf16(__builtin_bit_cast(bf16x2, k), __builtin_bit_cast(bf16x2, q), acc, false);
}
template <int CTRL> __device__ __forceinline__ float dpp(float x) {
  return __builtin_bit_cast(float, __builtin_amdgcn_mov_dpp(__builtin_bit_cast(int, x), CTRL, 0xf, 0xf, true));
}
__device__ __forceinline__ float row_sum(float x) {
  x += dpp<0x128>(x); x += dpp<0x124>(x); x += dpp<0x122>(x); x += dpp<0x121>(x); return x;
}
__device__ __forceinline__ float fold32(float a, float b) {
  auto s = __builtin_amdgcn_permlane32_swap(__float_as_uint(a), __float_as_uint(b), false, false);
  return __uint_as_float(s[0]) + __uint_as_float(s[1]);
}
__device__ __forceinline__ float fold16(float a, float b) {
  auto s = __builtin_amdgcn_permlane16_swap(__float_as_uint(a), __float_as_uint(b), false, false);
  return __uint_as_float(s[0]) + __uint_as_float(s[1]);
}
__device__ __forceinline__ float xrow_sum(float x) { x = fold16(x, x); return fold32(x, x); }
__device__ __forceinline__ float xrow_max(float x) {
  auto s = __builtin_amdgcn_permlane16_swap(__float_as_uint(x), __float_as_uint(x), false, false);
  x = fmaxf(__uint_as_float(s[0]), __uint_as_float(s[1]));
  auto t = __builtin_amdgcn_permlane32_swap(__float_as_uint(x), __float_as_uint(x), false, false);
  return fmaxf(__uint_as_float(t[0]), __uint_as_float(t[1]));
}
__device__ __forceinline__ float wave_sum(float x) { return xrow_sum(row_sum(x)); }
typedef __amdgpu_buffer_rsrc_t rsrc_t;
__device__ __forceinline__ rsrc_t mkrs(const void* p, unsigned bytes) { return __builtin_amdgcn_make_buffer_rsrc((void*)p, 0, (int)bytes, 0x00020000); }
#define BLD(rs_, vo_, so_, aux_) __builtin_amdgcn_raw_buffer_load_b128(rs_, vo_, so_, aux_)
__device__ __forceinline__ u64 ldg(const u64* p) { return __hip_atomic_load((gu64*)p, __ATOMIC_RELAXED, __HIP_MEMORY_SCOPE_AGENT); }
__device__ __forceinline__ void stg(u64* p, u64 v) { __hip_atomic_store((gu64*)p, v, __ATOMIC_RELAXED, __HIP_MEMORY_SCOPE_AGENT); }
__device__ __forceinline__ float silu(float x) { return x * __builtin_amdgcn_rcpf(1.f + __builtin_amdgcn_exp2f(-1.4426950408889634f * x)); }
#define CBAR() asm volatile("" ::: "memory")
#define SB() __builtin_amdgcn_sched_barrier(0)
__device__ __forceinline__ void fma8(float* a, float s, u32x4 w) {
#pragma unroll
  for (int k = 0; k < 4; ++k) { a[2 * k] = fmaf(s, bflo(w[k]), a[2 * k]); a[2 * k + 1] = fmaf(s, bfhi(w[k]), a[2 * k + 1]); }
}
__device__ __forceinline__ void fold8(const float* a, float& o0, float& o1) {
  const float u0 = fold32(a[0], a[4]), u1 = fold32(a[1], a[5]), u2 = fold32(a[2], a[6]), u3 = fold32(a[3], a[7]);
  o0 = fold16(u0, u2); o1 = fold16(u1, u3);
}
__device__ __forceinline__ unsigned nosent(float v) { const unsigned u = __float_as_uint(v); return u == SENT ? 0x7FC00000u : u; }

#define TSW(i, wv) do {} while (0)
#define TSR(i, wv) do {} while (0)
#define TS(i) TSW(i, 0)

#define BLD2(rs_, vo_, so_) __builtin_amdgcn_raw_buffer_load_b64(rs_, vo_, so_, 0)
#define BLD1(rs_, vo_, so_) __builtin_amdgcn_raw_buffer_load_b32(rs_, vo_, so_, 0)
constexpr int OOB = 0x7fffff00;
#define PAIRUP(a_, b_) do { const unsigned x_ = (a_), y_ = (b_); a_ = __builtin_amdgcn_perm(y_, x_, 0x05040100u); b_ = __builtin_amdgcn_perm(y_, x_, 0x07060302u); } while (0)
#define WAITV(n) asm volatile("s_waitcnt vmcnt(" #n ")" ::: "memory")
#define LDSWAIT() __builtin_amdgcn_s_waitcnt(0xC07F)
#define BAR() do { LDSWAIT(); __builtin_amdgcn_s_barrier(); CBAR(); } while (0)
#define TRS(HALF, CTRL) _Pragma("unroll") for (int i_ = 0; i_ < (HALF); ++i_) { const float lo_ = t[i_], hi_ = t[i_ + (HALF)]; \
    const float a_ = lo_ + dpp<CTRL>(lo_), b_ = hi_ + dpp<CTRL>(hi_); t[i_] = (c & (HALF)) ? b_ : a_; }

__device__ __forceinline__ void mod_chunk(const u32x4 w0, const u32x4 w1, const unsigned* cv, float* part, unsigned* cnt, u64* modg_all, int col, u64 tag,
                                          bool publish, int w, int lane, int c) {
  float u[32];
  float wa[8], wb[8];
#pragma unroll
  for (int k = 0; k < 4; ++k) { wa[2 * k] = bflo(w0[k]); wa[2 * k + 1] = bfhi(w0[k]); wb[2 * k] = bflo(w1[k]); wb[2 * k + 1] = bfhi(w1[k]); }
#pragma unroll
  for (int bb = 0; bb < 4; ++bb) {
    const float s0 = rbf(silu(bflo(cv[bb]))), s1 = rbf(silu(bfhi(cv[bb]))), z0 = rbf(silu(bflo(cv[bb + 4]))), z1 = rbf(silu(bfhi(cv[bb + 4])));
#pragma unroll
    for (int e = 0; e < 8; ++e) u[bb * 8 + e] = fold32(fmaf(s0, wa[e], s1 * wb[e]), fmaf(z0, wa[e], z1 * wb[e]));
  }
  float t[16];
#pragma unroll
  for (int i = 0; i < 16; ++i) t[i] = fold16(u[i], u[i + 16]);
  TRS(8, 0x128) TRS(4, 0x141) TRS(2, 0x4E) TRS(1, 0xB1)
  part[w * 64 + lane] = t[0];
  LDSWAIT();
  unsigned old = 0;
  if (lane == 0) old = __hip_atomic_fetch_add(cnt, 1u, __ATOMIC_RELAXED, __HIP_MEMORY_SCOPE_WORKGROUP);
  old = __builtin_amdgcn_readfirstlane(old);
  CBAR();
  if (old == 4u && publish) {
    const float v = ((part[lane] + part[64 + lane]) + (part[128 + lane] + part[192 + lane])) + part[256 + lane];
    stg(modg_all + (size_t)(lane >> 3) * NMOD + col + (lane & 7), tag | (u64)(cvtpk(v, 0.f) & 0xffffu));
  }
  CBAR();
}

__global__ void __launch_bounds__(NT, 2)
fused_block(const u16* X, const u16* Kc, const u16* Vc, const u16* Wq, const u16* Wo, const u16* W1, const u16* W2, const u16* Cc,
            const u16* Wm, u16* Out, unsigned char* ws) {
  __shared__ __attribute__((aligned(16))) unsigned w1s[DM * 32];
  __shared__ __attribute__((aligned(16))) float red[NW * DM];
  __shared__ __attribute__((aligned(16))) float vec[DM];
  __shared__ __attribute__((aligned(16))) float modr[4 * DM];
  __shared__ __attribute__((aligned(16))) float mrg[NW * (DH + 4)];
  __shared__ __attribute__((aligned(16))) float part[2 * 320];
  __shared__ __attribute__((aligned(16))) unsigned hp[DM / 2];
  __shared__ __attribute__((aligned(16))) u16 ob[DH];
  __shared__ unsigned ctr[4];
  const int tid = threadIdx.x, lane = tid & 63, w = __builtin_amdgcn_readfirstlane(tid >> 6), r = lane >> 4, c = lane & 15;
  const int bid = blockIdx.x, b = bid >> 5, h = bid & 31;
  TSW(0, 6); TSR(16, 6);
  u64* modg_all = reinterpret_cast<u64*>(ws + WS_MOD);
  const rsrc_t rs = mkrs(ws, (unsigned)WS_END);
  const unsigned p1off = (unsigned)WS_P1 + (unsigned)b * NH * DM * 4, p2off = (unsigned)WS_P2 + (unsigned)b * NH * DM * 4;
  const unsigned go = (unsigned)WS_MOD + (unsigned)b * NMOD * 8;
  if (w == 5) {
    const u32x4 sv = {SENT, SENT, SENT, SENT};
#pragma unroll
    for (int j = 0; j < 3; ++j) if (lane + 64 * j < DM / 4) {
      __builtin_amdgcn_raw_buffer_store_b128(sv, rs, p1off + h * DM * 4 + (lane + 64 * j) * 16, 0, 16);
      __builtin_amdgcn_raw_buffer_store_b128(sv, rs, p2off + h * DM * 4 + (lane + 64 * j) * 16, 0, 16);
    }
    if (lane < 4) ctr[lane] = lane == 2 ? 14u : 0u;
  }
  CBAR();
  const u64 epoch = *(const __attribute__((address_space(4))) u64*)(ws + WS_EPOCH);
  const int xs = bid & 7, xi = bid >> 3;
  const int colA = 8 * (xi < 20 ? xs * 20 + xi : 416 + xs * 8 + ((xi - 20) & 7));
  const int colB = 8 * (160 + xs * 32 + xi);
  const rsrc_t rWm = mkrs(Wm, DM * NMOD * 2);
  const rsrc_t rC = mkrs(Cc, NB * DM * 2);
  const rsrc_t rX = mkrs(X + b * DM, DM * 2);
  const int wmo = w < 5 ? (w * 128 + 2 * lane) * (NMOD * 2) : OOB;
  u32x4 wm0 = BLD(rWm, wmo, colA * 2, 0), wm1 = BLD(rWm, wmo, colA * 2 + NMOD * 2, 0);
  unsigned cv[8];
#pragma unroll
  for (int i = 0; i < 8; ++i) cv[i] = BLD1(rC, w < 5 ? w * 256 + lane * 4 : OOB, i * DM * 2);
  unsigned xv[5];
#pragma unroll
  for (int i = 0; i < 5; ++i) xv[i] = BLD1(rX, w == 7 ? lane * 4 : OOB, i * 256);
  const unsigned xa = BLD1(rX, tid * 4, 0);
  const rsrc_t rWq = mkrs(Wq + h * DH, (DM * NH * DH - h * DH) * 2);
  const unsigned tq = BLD1(rWq, tid < 320 ? (80 * b + (tid >> 2)) * (NH * DH * 2) + (tid & 3) * 64 : OOB, 0);
  BAR();
  u32x4 Kr[2][4], Vr[2][4];
  const rsrc_t rK = mkrs(Kc + (size_t)bid * SK * DH, SK * DH * 2), rV = mkrs(Vc + (size_t)bid * SK * DH, SK * DH * 2);
  const int kvo = lane * 16;
#define ISSUE(s_, i_) do { const unsigned ti_ = (unsigned)(i_) * (16 * DH * 2); \
    _Pragma("unroll") for (int t_ = 0; t_ < 4; ++t_) { Kr[s_][t_] = BLD(rK, kvo, ti_ + t_ * 1024, KVAUX); Vr[s_][t_] = BLD(rV, kvo, ti_ + t_ * 1024, KVAUX); } } while (0)
  const int fs = h < NFW ? h : 0;
  const int rg = w * 4 + r;
  unsigned i0 = w < 7 ? w : 4096u, i1 = w < 7 ? 7 + w : 4096u;
  ISSUE(0, i0);
  CBAR();
  const u64 tag = ((epoch + 1) & 0xFFFFFFFFFFFFull) << 16;
  const unsigned tlo = (unsigned)(tag >> 16) & 0xffffu, thi = (unsigned)(tag >> 32);
  asm volatile("" :: "v"(tq) : "memory");
  TSW(9, 1);
  if (w < 5) {
    mod_chunk(wm0, wm1, cv, part, &ctr[0], modg_all, colA, tag, xi < 28, w, lane, c);
    TS(1);
  }
  unsigned q[4];
  {
    u32x4 wq[20];
#pragma unroll
    for (int j = 0; j < 20; ++j) wq[j] = BLD(rWq, rg * 2 * (NH * DH * 2) + c * 16, ((j >> 1) * 64 + (j & 1)) * (NH * DH * 2), 0);
    CBAR();
    if (w == 7) {
      float mean = 0.f;
#pragma unroll
      for (int i = 0; i < 5; ++i) mean += bflo(xv[i]) + bfhi(xv[i]);
      mean = wave_sum(mean) * (1.f / DM);
      float var = 0.f;
#pragma unroll
      for (int i = 0; i < 5; ++i) { const float d0 = bflo(xv[i]) - mean, d1 = bfhi(xv[i]) - mean; var = fmaf(d0, d0, fmaf(d1, d1, var)); }
      var = wave_sum(var) * (1.f / DM);
      const float rstd = __builtin_amdgcn_rsqf(var + 1e-5f);
      u32x4 g[10];
      for (unsigned spins = 0;; ++spins) {
#pragma unroll
        for (int i = 0; i < 10; ++i) g[i] = BLD(rs, lane * 16, go + i * 1024, 16);
        bool ok = true;
#pragma unroll
        for (int i = 0; i < 10; ++i) ok &= (g[i][0] >> 16) == tlo && g[i][1] == thi && (g[i][2] >> 16) == tlo && g[i][3] == thi;
        if (__all(ok) || spins > SPIN_CAP) break;
        CBAR();
      }
#pragma unroll
      for (int i = 0; i < 5; ++i)
        hp[lane + 64 * i] = cvtpk((bflo(xv[i]) - mean) * rstd * (1.f + bflo(g[5 + i][0])) + bflo(g[i][0]),
                                  (bfhi(xv[i]) - mean) * rstd * (1.f + bflo(g[5 + i][2])) + bflo(g[i][2]));
      TSW(2, 7);
    }
    WAITV(0);
    TSW(13, 6);
    ISSUE(1, i1);
    CBAR();
#pragma unroll
    for (int j = 0; j < 10; ++j)
#pragma unroll
      for (int k = 0; k < 4; ++k) PAIRUP(wq[2 * j][k], wq[2 * j + 1][k]);
#pragma unroll
    for (int j = 0; j < 20; j += 2) asm volatile("" : "+v"(wq[j]), "+v"(wq[j + 1]));
    BAR();
    float a[8] = {0.f, 0.f, 0.f, 0.f, 0.f, 0.f, 0.f, 0.f};
#pragma unroll
    for (int j = 0; j < 10; ++j) {
      const unsigned hv = hp[j * 32 + rg];
#pragma unroll
      for (int k = 0; k < 4; ++k) { a[2 * k] = dot2(wq[2 * j][k], hv, a[2 * k]); a[2 * k + 1] = dot2(wq[2 * j + 1][k], hv, a[2 * k + 1]); }
    }
    float o0, o1; fold8(a, o0, o1);
    *reinterpret_cast<f32x2*>(red + w * DH + 8 * c + 2 * r) = f32x2{o0, o1};
    BAR();
    float s0 = 0.f, s1 = 0.f;
#pragma unroll
    for (int u = 0; u < NW; ++u) { const f32x2 t = *reinterpret_cast<const f32x2*>(red + u * DH + 8 * c + 2 * r); s0 += t[0]; s1 += t[1]; }
    const unsigned pk = cvtpk(s0, s1);
    auto ab = __builtin_amdgcn_permlane16_swap(pk, pk, false, false);
    auto e0 = __builtin_amdgcn_permlane32_swap(ab[0], ab[0], false, false);
    auto e1 = __builtin_amdgcn_permlane32_swap(ab[1], ab[1], false, false);
    q[0] = e0[0]; q[2] = e0[1]; q[1] = e1[0]; q[3] = e1[1];
  }
  TS(3);
  wm0 = BLD(rWm, wmo, colB * 2, 0); wm1 = BLD(rWm, wmo, colB * 2 + NMOD * 2, 0);
  const rsrc_t rW1 = mkrs(W1, DM * FF * 2);
  const rsrc_t rWo = mkrs(Wo + (size_t)h * DH * DM, DH * DM * 2);
  const rsrc_t rW2 = mkrs(W2 + (size_t)fs * 64 * DM, 64 * DM * 2);
  const unsigned to = BLD1(rWo, tid < 320 ? (b * 320 + tid) * 64 : OOB, 0);
  const unsigned t1 = BLD1(rW1, tid >= 320 && tid < 480 ? (80 * b + ((tid - 320) >> 1)) * (FF * 2) + fs * 128 + (tid & 1) * 64 : OOB, 0);
  const unsigned t2 = BLD1(rW2, tid < 160 ? (b * 160 + tid) * 64 : OOB, 0);
  CBAR();
  constexpr float C2 = 0.088388347648318440f * 1.4426950408889634f;
  float m = -3.0e38f, l = 0.f, acc[8] = {0.f, 0.f, 0.f, 0.f, 0.f, 0.f, 0.f, 0.f};
#define PROC(s_) do { float s4_[4]; \
    _Pragma("unroll") for (int t_ = 0; t_ < 4; ++t_) { float d_ = 0.f; \
      _Pragma("unroll") for (int k_ = 0; k_ < 4; ++k_) d_ = dot2(Kr[s_][t_][k_], q[k_], d_); \
      s4_[t_] = row_sum(d_) * C2; } \
    const float mn_ = fmaxf(fmaxf(fmaxf(s4_[0], s4_[1]), fmaxf(s4_[2], s4_[3])), m); \
    const float al_ = __builtin_amdgcn_exp2f(m - mn_); m = mn_; \
    _Pragma("unroll") for (int t_ = 0; t_ < 4; ++t_) s4_[t_] = __builtin_amdgcn_exp2f(s4_[t_] - mn_); \
    l = fmaf(l, al_, (s4_[0] + s4_[1]) + (s4_[2] + s4_[3])); \
    _Pragma("unroll") for (int k_ = 0; k_ < 8; ++k_) acc[k_] *= al_; \
    _Pragma("unroll") for (int t_ = 0; t_ < 4; ++t_) fma8(acc, s4_[t_], Vr[s_][t_]); \
    asm volatile("" : "+v"(acc[0]), "+v"(acc[1]), "+v"(acc[2]), "+v"(acc[3]), "+v"(acc[4]), "+v"(acc[5]), "+v"(acc[6]), "+v"(acc[7]), "+v"(l), "+v"(m) :: "memory"); } while (0)
#define STEP(s_, i_) do { if (i_ < 256u) PROC(s_); SB(); unsigned o_ = 0; \
    if (lane == 0) o_ = __hip_atomic_fetch_add(&ctr[2], 1u, __ATOMIC_RELAXED, __HIP_MEMORY_SCOPE_WORKGROUP); \
    i_ = __builtin_amdgcn_readfirstlane(o_); ISSUE(s_, i_); SB(); } while (0)
#define ROUNDS(n_) for (int it_ = 0; it_ < (n_); ++it_) { STEP(0, i0); STEP(1, i1); }
  ROUNDS(1);
  CBAR();
  if (w < 5) mod_chunk(wm0, wm1, cv, part + 320, &ctr[1], modg_all, colB, tag, true, w, lane, c);
  asm volatile("" :: "v"(to), "v"(t1), "v"(t2) : "memory");
  {
    u32x4 t[10];
#pragma unroll
    for (int i = 0; i < 10; ++i) t[i] = BLD(rW1, (lane >> 3) * (2 * FF * 2) + (lane & 7) * 16, (w * 80 + (i >> 1) * 16 + (i & 1)) * (FF * 2) + fs * 128, 0);
    CBAR();
    ROUNDS(1);
    CBAR();
#pragma unroll
    for (int i = 0; i < 5; ++i) {
      const int pr = w * 40 + i * 8 + (lane >> 3);
#pragma unroll
      for (int k = 0; k < 4; ++k) PAIRUP(t[2 * i][k], t[2 * i + 1][k]);
      *reinterpret_cast<u32x4*>(w1s + pr * 64 + (((2 * (lane & 7)) ^ (pr & 15)) * 4)) = u32x4{t[2 * i][0], t[2 * i + 1][0], t[2 * i][1], t[2 * i + 1][1]};
      *reinterpret_cast<u32x4*>(w1s + pr * 64 + (((2 * (lane & 7) + 1) ^ (pr & 15)) * 4)) = u32x4{t[2 * i][2], t[2 * i + 1][2], t[2 * i][3], t[2 * i + 1][3]};
    }
  }
  CBAR();
  const int hf = lane >> 5, l32 = lane & 31;
  u32x2 wo[40], w2[20];
#pragma unroll
  for (int i = 0; i < 8; ++i)
#pragma unroll
    for (int jb = 0; jb < 5; ++jb) wo[i * 5 + jb] = BLD2(rWo, hf * 8 * (DM * 2) + l32 * 8, (16 * w + i) * (DM * 2) + jb * 256);
  CBAR();
  ROUNDS(1);
  CBAR();
#pragma unroll
  for (int i = 0; i < 4; ++i)
#pragma unroll
    for (int jb = 0; jb < 5; ++jb) w2[i * 5 + jb] = BLD2(rW2, hf * 4 * (DM * 2) + l32 * 8, (8 * w + i) * (DM * 2) + jb * 256);
  CBAR();
#pragma unroll
  for (int i = 0; i < 8; i += 2)
#pragma unroll
    for (int jb = 0; jb < 5; ++jb) { PAIRUP(wo[i * 5 + jb][0], wo[i * 5 + 5 + jb][0]); PAIRUP(wo[i * 5 + jb][1], wo[i * 5 + 5 + jb][1]);
      asm volatile("" : "+v"(wo[i * 5 + jb]), "+v"(wo[i * 5 + 5 + jb])); }
  ROUNDS(2);
  CBAR();
#pragma unroll
  for (int i = 0; i < 4; i += 2)
#pragma unroll
    for (int jb = 0; jb < 5; ++jb) { PAIRUP(w2[i * 5 + jb][0], w2[i * 5 + 5 + jb][0]); PAIRUP(w2[i * 5 + jb][1], w2[i * 5 + 5 + jb][1]);
      asm volatile("" : "+v"(w2[i * 5 + jb]), "+v"(w2[i * 5 + 5 + jb])); }
  {
    u32x4 g[3];
    for (unsigned spins = 0;; ++spins) {
#pragma unroll
      for (int j = 0; j < 3; ++j) g[j] = BLD(rs, (j < 2 || tid < 256 ? tid : 0) * 16, go + 2 * DM * 8 + j * NT * 16, 16);
      CBAR();
      ROUNDS(1);
      bool ok = true;
#pragma unroll
      for (int j = 0; j < 3; ++j) ok &= (g[j][0] >> 16) == tlo && g[j][1] == thi && (g[j][2] >> 16) == tlo && g[j][3] == thi;
      if (__all(ok) || spins > SPIN_CAP) break;
    }
#pragma unroll
    for (int j = 0; j < 3; ++j) if (j < 2 || tid < 256) {
      const int k = 2 * (tid + NT * j);
      const float add = (k >= 2 * DM && k < 3 * DM) ? 1.f : 0.f;
      *reinterpret_cast<f32x2*>(modr + k) = f32x2{bflo(g[j][0]) + add, bflo(g[j][2]) + add};
    }
  }
  CBAR();
  do { STEP(0, i0); STEP(1, i1); } while (i0 < 256u);
  CBAR();
  TS(4);
  {
    const float M = xrow_max(m), f = __builtin_amdgcn_exp2f(m - M);
    l = xrow_sum(l * f);
#pragma unroll
    for (int k = 0; k < 8; ++k) acc[k] *= f;
    float o0, o1; fold8(acc, o0, o1);
    *reinterpret_cast<f32x2*>(mrg + w * (DH + 4) + 4 + 8 * c + 2 * r) = f32x2{o0, o1};
    if (lane == 0) *reinterpret_cast<f32x2*>(mrg + w * (DH + 4)) = f32x2{M, l};
  }
  BAR();
  TSW(10, 0);
  {
    f32x2 ml[NW]; float av[NW];
#pragma unroll
    for (int u = 0; u < NW; ++u) { ml[u] = *reinterpret_cast<const f32x2*>(mrg + u * (DH + 4)); av[u] = mrg[u * (DH + 4) + 4 + 16 * w + c]; }
    float M = ml[0][0];
#pragma unroll
    for (int u = 1; u < NW; ++u) M = fmaxf(M, ml[u][0]);
    float L = 0.f, ov = 0.f;
#pragma unroll
    for (int u = 0; u < NW; ++u) { const float f = __builtin_amdgcn_exp2f(ml[u][0] - M); L = fmaf(f, ml[u][1], L); ov = fmaf(f, av[u], ov); }
    if (lane < 16) ob[16 * w + c] = (u16)cvtpk(ov * __builtin_amdgcn_rcpf(L), 0.f);
    LDSWAIT(); CBAR();
    const u32x4 op = *reinterpret_cast<const u32x4*>(ob + 16 * w + 8 * hf);
#pragma unroll
    for (int jb = 0; jb < 5; ++jb) {
      float a[4] = {0.f, 0.f, 0.f, 0.f};
#pragma unroll
      for (int p = 0; p < 4; ++p) {
        a[0] = dot2(wo[p * 10 + jb][0], op[p], a[0]); a[1] = dot2(wo[p * 10 + 5 + jb][0], op[p], a[1]);
        a[2] = dot2(wo[p * 10 + jb][1], op[p], a[2]); a[3] = dot2(wo[p * 10 + 5 + jb][1], op[p], a[3]);
      }
      *reinterpret_cast<f32x2*>(red + w * DM + jb * 128 + 4 * l32 + 2 * hf) = f32x2{fold32(a[0], a[2]), fold32(a[1], a[3])};
    }
  }
  BAR();
  if (tid < DM / 4) {
    f32x4 s = *reinterpret_cast<const f32x4*>(red + tid * 4);
#pragma unroll
    for (int u = 1; u < NW; ++u) s += *reinterpret_cast<const f32x4*>(red + u * DM + tid * 4);
    const u32x4 o = {nosent(s[0]), nosent(s[1]), nosent(s[2]), nosent(s[3])};
    __builtin_amdgcn_raw_buffer_store_b128(o, rs, p1off + h * DM * 4 + tid * 16, 0, 16);
  }
  CBAR();
  TS(5);
  {
    f32x4 s[3];
    const int c2 = lane + 128 < DM / 4 ? lane + 128 : lane;
    for (unsigned spins = 0;; ++spins) {
      u32x4 g[12];
#pragma unroll
      for (int p = 0; p < 4; ++p) {
        const unsigned base = p1off + (4 * w + p) * DM * 4;
        g[p * 3 + 0] = BLD(rs, lane * 16, base, 16);
        g[p * 3 + 1] = BLD(rs, lane * 16, base + 1024, 16);
        g[p * 3 + 2] = BLD(rs, c2 * 16, base, 16);
      }
      unsigned mn = 0;
#pragma unroll
      for (int k = 0; k < 12; ++k) mn = max(max(mn, max(g[k][0], g[k][1])), max(g[k][2], g[k][3]));
#pragma unroll
      for (int k = 0; k < 3; ++k)
        s[k] = (__builtin_bit_cast(f32x4, g[k]) + __builtin_bit_cast(f32x4, g[3 + k])) + (__builtin_bit_cast(f32x4, g[6 + k]) + __builtin_bit_cast(f32x4, g[9 + k]));
      if (__all(mn != SENT) || spins > SPIN_CAP) break;
      __builtin_amdgcn_s_sleep(2);
      CBAR();
    }
    BAR();
    *reinterpret_cast<f32x4*>(red + w * DM + lane * 4) = s[0];
    *reinterpret_cast<f32x4*>(red + w * DM + (lane + 64) * 4) = s[1];
    if (lane < 32) *reinterpret_cast<f32x4*>(red + w * DM + (lane + 128) * 4) = s[2];
  }
  TS(6);
  BAR();
  if (tid < DM / 2) {
    f32x2 s = *reinterpret_cast<const f32x2*>(red + tid * 2);
#pragma unroll
    for (int u = 1; u < NW; ++u) s += *reinterpret_cast<const f32x2*>(red + u * DM + tid * 2);
    const f32x2 ga = *reinterpret_cast<const f32x2*>(modr + tid * 2);
    const unsigned sb2 = cvtpk(s[0], s[1]);
    const unsigned pr2 = cvtpk(ga[0] * bflo(sb2), ga[1] * bfhi(sb2));
    const unsigned x2 = cvtpk(bflo(xa) + bflo(pr2), bfhi(xa) + bfhi(pr2));
    *reinterpret_cast<f32x2*>(vec + tid * 2) = f32x2{bflo(x2), bfhi(x2)};
  }
  BAR();
  if (h < NFW) {
    f32x2 x1[5], sb[5], cb[5];
    u32x4 wa[5], wb[5];
#pragma unroll
    for (int i = 0; i < 5; ++i) x1[i] = *reinterpret_cast<const f32x2*>(vec + 2 * lane + 128 * i);
#pragma unroll
    for (int i = 0; i < 5; ++i) {
      sb[i] = *reinterpret_cast<const f32x2*>(modr + DM + 2 * lane + 128 * i); cb[i] = *reinterpret_cast<const f32x2*>(modr + 2 * DM + 2 * lane + 128 * i);
      wa[i] = *reinterpret_cast<const u32x4*>(w1s + (lane + 64 * i) * 64 + (((2 * w) ^ c) * 4));
      wb[i] = *reinterpret_cast<const u32x4*>(w1s + (lane + 64 * i) * 64 + (((2 * w + 1) ^ c) * 4));
    }
    float mu = 0.f;
#pragma unroll
    for (int i = 0; i < 5; ++i) mu += x1[i][0] + x1[i][1];
    mu = wave_sum(mu) * (1.f / DM);
    float vr = 0.f;
#pragma unroll
    for (int i = 0; i < 5; ++i) { x1[i] -= mu; vr = fmaf(x1[i][0], x1[i][0], fmaf(x1[i][1], x1[i][1], vr)); }
    vr = wave_sum(vr) * (1.f / DM);
    const float rs2 = __builtin_amdgcn_rsqf(vr + 1e-5f);
    float a[8] = {0.f, 0.f, 0.f, 0.f, 0.f, 0.f, 0.f, 0.f};
#pragma unroll
    for (int i = 0; i < 5; ++i) {
      const unsigned h2 = cvtpk(fmaf(x1[i][0] * rs2, cb[i][0], sb[i][0]), fmaf(x1[i][1] * rs2, cb[i][1], sb[i][1]));
#pragma unroll
      for (int k = 0; k < 4; ++k) { a[k] = dot2(wa[i][k], h2, a[k]); a[4 + k] = dot2(wb[i][k], h2, a[4 + k]); }
    }
    float o0, o1; fold8(a, o0, o1); o0 = silu(rbf(row_sum(o0))); o1 = silu(rbf(row_sum(o1)));
    const unsigned pk = cvtpk(o0, o1);
    auto ab = __builtin_amdgcn_permlane16_swap(pk, pk, false, false);
#pragma unroll
    for (int jb = 0; jb < 5; ++jb) {
      float a2[4] = {0.f, 0.f, 0.f, 0.f};
#pragma unroll
      for (int p = 0; p < 2; ++p) {
        a2[0] = dot2(w2[p * 10 + jb][0], ab[p], a2[0]); a2[1] = dot2(w2[p * 10 + 5 + jb][0], ab[p], a2[1]);
        a2[2] = dot2(w2[p * 10 + jb][1], ab[p], a2[2]); a2[3] = dot2(w2[p * 10 + 5 + jb][1], ab[p], a2[3]);
      }
      *reinterpret_cast<f32x2*>(red + w * DM + jb * 128 + 4 * l32 + 2 * hf) = f32x2{fold32(a2[0], a2[2]), fold32(a2[1], a2[3])};
    }
    BAR();
    if (tid < DM / 4) {
      f32x4 s = *reinterpret_cast<const f32x4*>(red + tid * 4);
#pragma unroll
      for (int u = 1; u < NW; ++u) s += *reinterpret_cast<const f32x4*>(red + u * DM + tid * 4);
      const u32x4 o = {nosent(s[0]), nosent(s[1]), nosent(s[2]), nosent(s[3])};
      __builtin_amdgcn_raw_buffer_store_b128(o, rs, p2off + h * DM * 4 + tid * 16, 0, 16);
    }
    CBAR();
  }
  TS(7);
  if (w == 3) {
    const int ch = lane % 5, pg = lane / 5;
    f32x4 s;
    for (unsigned spins = 0;; ++spins) {
      u32x4 g[3];
      bool ok = true;
      s = f32x4{0.f, 0.f, 0.f, 0.f};
#pragma unroll
      for (int k = 0; k < 3; ++k) {
        const int p = pg + 12 * k; const bool live = lane < 60 && p < NFW;
        g[k] = __builtin_amdgcn_raw_buffer_load_b128(rs, p2off + (live ? p : 0) * DM * 4 + (h * 5 + ch) * 16, 0, 16);
      }
#pragma unroll
      for (int k = 0; k < 3; ++k) {
        const int p = pg + 12 * k; const bool live = lane < 60 && p < NFW;
        ok &= g[k][0] != SENT && g[k][1] != SENT && g[k][2] != SENT && g[k][3] != SENT;
        if (live) s += __builtin_bit_cast(f32x4, g[k]);
      }
      if (__all(ok) || spins > SPIN_CAP) break;
      __builtin_amdgcn_s_sleep(2);
      CBAR();
    }
    *reinterpret_cast<f32x4*>(mrg + lane * 4) = s;
    __builtin_amdgcn_s_waitcnt(0xC07F);
    CBAR();
    if (lane < 20) {
      float t = 0.f;
#pragma unroll
      for (int u = 0; u < 12; ++u) t += mrg[u * 20 + lane];
      const int col = h * 20 + lane;
      const float o = vec[col] + rbf(modr[3 * DM + col] * rbf(t));
      Out[b * DM + col] = (u16)(cvtpk(o, 0.f) & 0xffffu);
    }
    if (bid == 0 && lane == 0) stg(reinterpret_cast<u64*>(ws + WS_EPOCH), epoch + 1);
    TSW(8, 3); TSR(17, 3);
  }
}

extern "C" void kernel_launch(void* const* d_in, const int* in_sizes, int n_in, void* d_out, int out_size, void* d_ws, size_t ws_size,
                              hipStream_t stream) {
  (void)in_sizes; (void)n_in; (void)out_size; (void)ws_size;
  hipLaunchKernelGGL(fused_block, dim3(NB * NH), dim3(NT), 0, stream, (const u16*)d_in[0], (const u16*)d_in[1], (const u16*)d_in[2],
                     (const u16*)d_in[3], (const u16*)d_in[4], (const u16*)d_in[6], (const u16*)d_in[7], (const u16*)d_in[8],
                     (const u16*)d_in[9], (u16*)d_out, (unsigned char*)d_ws);
}
```

```cpp
#include <hip/hip_runtime.h>
#include <hip/hip_bf16.h>
#include <stdint.h>

typedef unsigned short u16;
typedef unsigned long long u64;
using u32x4 = __attribute__((ext_vector_type(4))) unsigned;
using u32x2 = __attribute__((ext_vector_type(2))) unsigned;
using f32x4 = __attribute__((ext_vector_type(4))) float;
using f32x2 = __attribute__((ext_vector_type(2))) float;
using bf16x2 = __attribute__((ext_vector_type(2))) __bf16;
typedef __attribute__((address_space(1))) u64 gu64;

constexpr int NB = 8, DM = 640, NH = 32, SK = 4096, DH = 128, FF = 1728, NMOD = 6 * DM;
constexpr int NT = 512, NW = 8;
constexpr int NFW = FF / 64;
constexpr size_t WS_EPOCH = 0;
constexpr size_t WS_MOD = 4096;
constexpr size_t WS_P1 = WS_MOD + (size_t)NB * NMOD * 8;
constexpr size_t WS_P2 = WS_P1 + (size_t)NB * NH * DM * 4;
constexpr size_t WS_END = WS_P2 + (size_t)NB * NH * DM * 4;
constexpr size_t WS_DBG = 4u << 20;
constexpr unsigned SENT = 0xFFFFFFFFu;
constexpr unsigned SPIN_CAP = 1u << 22;

#define KVAUX 2
#define SLOT3 1
#define PSLEEP 0
#define EARLY 0
#define PSL 2
#define STATIC 1
#define PACE 1

__device__ __forceinline__ float bf2f(unsigned v) { return __builtin_bit_cast(float, v << 16); }
__device__ __forceinline__ float bflo(unsigned v) { return __builtin_bit_cast(float, v << 16); }
__device__ __forceinline__ float bfhi(unsigned v) { return __builtin_bit_cast(float, v & 0xffff0000u); }
__device__ __forceinline__ unsigned cvtpk(float lo, float hi) {
  unsigned r; asm("v_cvt_pk_bf16_f32 %0, %1, %2" : "=v"(r) : "v"(lo), "v"(hi)); return r;
}
__device__ __forceinline__ float rbf(float x) { return bflo(cvtpk(x, x)); }
__device__ __forceinline__ float dot2(unsigned k, unsigned q, float acc) {
  return __builtin_amdgcn_fdot2_f32_bf16(__builtin_bit_cast(bf16x2, k), __builtin_bit_cast(bf16x2, q), acc, false);
}
template <int CTRL> __device__ __forceinline__ float dpp(float x) {
  return __builtin_bit_cast(float, __builtin_amdgcn_mov_dpp(__builtin_bit_cast(int, x), CTRL, 0xf, 0xf, true));
}
__device__ __forceinline__ float row_sum(float x) {
  x += dpp<0x128>(x); x += dpp<0x124>(x); x += dpp<0x122>(x); x += dpp<0x121>(x); return x;
}
__device__ __forceinline__ float fold32(float a, float b) {
  auto s = __builtin_amdgcn_permlane32_swap(__float_as_uint(a), __float_as_uint(b), false, false);
  return __uint_as_float(s[0]) + __uint_as_float(s[1]);
}
__device__ __forceinline__ float fold16(float a, float b) {
  auto s = __builtin_amdgcn_permlane16_swap(__float_as_uint(a), __float_as_uint(b), false, false);
  return __uint_as_float(s[0]) + __uint_as_float(s[1]);
}
__device__ __forceinline__ float xrow_sum(float x) { x = fold16(x, x); return fold32(x, x); }
__device__ __forceinline__ float xrow_max(float x) {
  auto s = __builtin_amdgcn_permlane16_swap(__float_as_uint(x), __float_as_uint(x), false, false);
  x = fmaxf(__uint_as_float(s[0]), __uint_as_float(s[1]));
  auto t = __builtin_amdgcn_permlane32_swap(__float_as_uint(x), __float_as_uint(x), false, false);
  return fmaxf(__uint_as_float(t[0]), __uint_as_float(t[1]));
}
__device__ __forceinline__ float wave_sum(float x) { return xrow_sum(row_sum(x)); }
typedef __amdgpu_buffer_rsrc_t rsrc_t;
typedef __attribute__((address_space(3))) void* lds_ptr;
__device__ __forceinline__ rsrc_t mkrs(const void* p, unsigned bytes) { return __builtin_amdgcn_make_buffer_rsrc((void*)p, 0, (int)bytes, 0x00020000); }
#define BLD(rs_, vo_, so_, aux_) __builtin_amdgcn_raw_buffer_load_b128(rs_, vo_, so_, aux_)
__device__ __forceinline__ u64 ldg(const u64* p) { return __hip_atomic_load((gu64*)p, __ATOMIC_RELAXED, __HIP_MEMORY_SCOPE_AGENT); }
__device__ __forceinline__ void stg(u64* p, u64 v) { __hip_atomic_store((gu64*)p, v, __ATOMIC_RELAXED, __HIP_MEMORY_SCOPE_AGENT); }
__device__ __forceinline__ float silu(float x) { return x * __builtin_amdgcn_rcpf(1.f + __builtin_amdgcn_exp2f(-1.4426950408889634f * x)); }
#define CBAR() asm volatile("" ::: "memory")
#define SB() __builtin_amdgcn_sched_barrier(0)
__device__ __forceinline__ void fma8(float* a, float s, u32x4 w) {
#pragma unroll
  for (int k = 0; k < 4; ++k) { a[2 * k] = fmaf(s, bflo(w[k]), a[2 * k]); a[2 * k + 1] = fmaf(s, bfhi(w[k]), a[2 * k + 1]); }
}
__device__ __forceinline__ void fold8(const float* a, float& o0, float& o1) {
  const float u0 = fold32(a[0], a[4]), u1 = fold32(a[1], a[5]), u2 = fold32(a[2], a[6]), u3 = fold32(a[3], a[7]);
  o0 = fold16(u0, u2); o1 = fold16(u1, u3);
}
__device__ __forceinline__ unsigned nosent(float v) { const unsigned u = __float_as_uint(v); return u == SENT ? 0x7FC00000u : u; }

#define TSW(i, wv) do {} while (0)
#define TSR(i, wv) do {} while (0)
#define TS(i) TSW(i, 0)

#define BLD2(rs_, vo_, so_) __builtin_amdgcn_raw_buffer_load_b64(rs_, vo_, so_, 0)
#define BLD1(rs_, vo_, so_) __builtin_amdgcn_raw_buffer_load_b32(rs_, vo_, so_, 0)
constexpr int OOB = 0x7fffff00;
#define PAIRUP(a_, b_) do { const unsigned x_ = (a_), y_ = (b_); a_ = __builtin_amdgcn_perm(y_, x_, 0x05040100u); b_ = __builtin_amdgcn_perm(y_, x_, 0x07060302u); } while (0)
#define WAITV(n) asm volatile("s_waitcnt vmcnt(" #n ")" ::: "memory")
#define LDSWAIT() __builtin_amdgcn_s_waitcnt(0xC07F)
#define BAR() do { LDSWAIT(); __builtin_amdgcn_s_barrier(); CBAR(); } while (0)
#define TRS(HALF, CTRL) _Pragma("unroll") for (int i_ = 0; i_ < (HALF); ++i_) { const float lo_ = t[i_], hi_ = t[i_ + (HALF)]; \
    const float a_ = lo_ + dpp<CTRL>(lo_), b_ = hi_ + dpp<CTRL>(hi_); t[i_] = (c & (HALF)) ? b_ : a_; }

__device__ __forceinline__ void mod_chunk(const u32x4 w0, const u32x4 w1, const unsigned* cv, float* part, unsigned* cnt, u64* modg_all, int col, u64 tag,
                                          bool publish, int w, int lane, int c) {
  float u[32];
  float wa[8], wb[8];
#pragma unroll
  for (int k = 0; k < 4; ++k) { wa[2 * k] = bflo(w0[k]); wa[2 * k + 1] = bfhi(w0[k]); wb[2 * k] = bflo(w1[k]); wb[2 * k + 1] = bfhi(w1[k]); }
#pragma unroll
  for (int bb = 0; bb < 4; ++bb) {
    const float s0 = rbf(silu(bflo(cv[bb]))), s1 = rbf(silu(bfhi(cv[bb]))), z0 = rbf(silu(bflo(cv[bb + 4]))), z1 = rbf(silu(bfhi(cv[bb + 4])));
#pragma unroll
    for (int e = 0; e < 8; ++e) u[bb * 8 + e] = fold32(fmaf(s0, wa[e], s1 * wb[e]), fmaf(z0, wa[e], z1 * wb[e]));
  }
  float t[16];
#pragma unroll
  for (int i = 0; i < 16; ++i) t[i] = fold16(u[i], u[i + 16]);
  TRS(8, 0x128) TRS(4, 0x141) TRS(2, 0x4E) TRS(1, 0xB1)
  part[w * 64 + lane] = t[0];
  LDSWAIT();
  unsigned old = 0;
  if (lane == 0) old = __hip_atomic_fetch_add(cnt, 1u, __ATOMIC_RELAXED, __HIP_MEMORY_SCOPE_WORKGROUP);
  old = __builtin_amdgcn_readfirstlane(old);
  CBAR();
  if (old == 4u && publish) {
    const float v = ((part[lane] + part[64 + lane]) + (part[128 + lane] + part[192 + lane])) + part[256 + lane];
    stg(modg_all + (size_t)(lane >> 3) * NMOD + col + (lane & 7), tag | (u64)(cvtpk(v, 0.f) & 0xffffu));
  }
  CBAR();
}

__global__ void __launch_bounds__(NT, 2)
fused_block(const u16* X, const u16* Kc, const u16* Vc, const u16* Wq, const u16* Wo, const u16* W1, const u16* W2, const u16* Cc,
            const u16* Wm, u16* Out, unsigned char* ws) {
  __shared__ __attribute__((aligned(16))) unsigned w1s[DM * 32];
  __shared__ __attribute__((aligned(16))) float red[7 * 2048];
  __shared__ __attribute__((aligned(16))) float vec[DM];
  __shared__ __attribute__((aligned(16))) float modr[4 * DM];
  __shared__ __attribute__((aligned(16))) float mrg[NW * (DH + 4)];
  __shared__ __attribute__((aligned(16))) float part[2 * 320];
  __shared__ __attribute__((aligned(16))) unsigned hp[DM / 2];
  __shared__ __attribute__((aligned(16))) u16 ob[DH];
  __shared__ unsigned ctr[4];
  const int tid = threadIdx.x, lane = tid & 63, w = __builtin_amdgcn_readfirstlane(tid >> 6), r = lane >> 4, c = lane & 15;
  const int bid = blockIdx.x, b = bid >> 5, h = bid & 31;
  TSW(0, 6); TSR(16, 6);
  u64* modg_all = reinterpret_cast<u64*>(ws + WS_MOD);
  const rsrc_t rs = mkrs(ws, (unsigned)WS_END);
  const unsigned p1off = (unsigned)WS_P1 + (unsigned)b * NH * DM * 4, p2off = (unsigned)WS_P2 + (unsigned)b * NH * DM * 4;
  const unsigned go = (unsigned)WS_MOD + (unsigned)b * NMOD * 8;
  if (w == 7) {
    const u32x4 sv = {SENT, SENT, SENT, SENT};
#pragma unroll
    for (int j = 0; j < 3; ++j) if (lane + 64 * j < DM / 4) {
      __builtin_amdgcn_raw_buffer_store_b128(sv, rs, p1off + h * DM * 4 + (lane + 64 * j) * 16, 0, 16);
      __builtin_amdgcn_raw_buffer_store_b128(sv, rs, p2off + h * DM * 4 + (lane + 64 * j) * 16, 0, 16);
    }
    if (lane < 4) ctr[lane] = lane == 2 ? (SLOT3 ? 53u : 45u) : 0u;
  }
  CBAR();
  const u64 epoch = *(const __attribute__((address_space(4))) u64*)(ws + WS_EPOCH);
  const int xs = bid & 7, xi = bid >> 3;
  const int colA = 8 * (xi < 20 ? xs * 20 + xi : 416 + xs * 8 + ((xi - 20) & 7));
  const int colB = 8 * (160 + xs * 32 + xi);
  const rsrc_t rWm = mkrs(Wm, DM * NMOD * 2);
  const rsrc_t rC = mkrs(Cc, NB * DM * 2);
  const rsrc_t rX = mkrs(X + b * DM, DM * 2);
  const int wmo = w < 5 ? (w * 128 + 2 * lane) * (NMOD * 2) : OOB;
  u32x4 wm0 = BLD(rWm, wmo, colA * 2, 0), wm1 = BLD(rWm, wmo, colA * 2 + NMOD * 2, 0);
  unsigned cv[8];
#pragma unroll
  for (int i = 0; i < 8; ++i) cv[i] = BLD1(rC, w < 5 ? w * 256 + lane * 4 : OOB, i * DM * 2);
  unsigned xv[5];
#pragma unroll
  for (int i = 0; i < 5; ++i) xv[i] = BLD1(rX, w == 7 ? lane * 4 : OOB, i * 256);
  const unsigned xa = BLD1(rX, tid * 4, 0);
  const rsrc_t rWq = mkrs(Wq + h * DH, (DM * NH * DH - h * DH) * 2);
  const unsigned tq = BLD1(rWq, tid < 320 ? (80 * b + (tid >> 2)) * (NH * DH * 2) + (tid & 3) * 64 : OOB, 0);
  BAR();
  u32x4 Kr[4][4], Vr[4][4];
  const rsrc_t rK = mkrs(Kc + (size_t)bid * SK * DH, SK * DH * 2), rV = mkrs(Vc + (size_t)bid * SK * DH, SK * DH * 2);
  const int kvo = lane * 16;
#define ISSUE(s_, i_) do { const unsigned ti_ = (unsigned)(i_) * (16 * DH * 2); \
    _Pragma("unroll") for (int t_ = 0; t_ < 4; ++t_) { Kr[s_][t_] = BLD(rK, kvo, ti_ + t_ * 1024, KVAUX); Vr[s_][t_] = BLD(rV, kvo, ti_ + t_ * 1024, KVAUX); } } while (0)
#define DMA1(rs_, dst_, o_) __builtin_amdgcn_raw_ptr_buffer_load_lds(rs_, (lds_ptr)(dst_), 16, kvo, ti_, o_, KVAUX)
#define DMA(dst_, i_) do { const unsigned ti_ = (unsigned)(i_) * (16 * DH * 2); \
    DMA1(rK, dst_, 0); DMA1(rK, dst_, 1024); DMA1(rK, dst_, 2048); DMA1(rK, dst_, 3072); \
    DMA1(rV, (dst_) + 1024, 0); DMA1(rV, (dst_) + 1024, 1024); DMA1(rV, (dst_) + 1024, 2048); DMA1(rV, (dst_) + 1024, 3072); } while (0)
#define FROMLDS(s_, src_) do { _Pragma("unroll") for (int t_ = 0; t_ < 4; ++t_) { Kr[s_][t_] = *reinterpret_cast<const u32x4*>((src_) + t_ * 256 + lane * 4); \
    Vr[s_][t_] = *reinterpret_cast<const u32x4*>((src_) + 1024 + t_ * 256 + lane * 4); } } while (0)
  const int fs = h < NFW ? h : 0;
  const int rg = w * 4 + r;
  unsigned i0 = w < 7 ? w : 4096u, i1 = w < 7 ? 7 + w : 4096u;
  ISSUE(0, i0);
  CBAR();
  const u64 tag = ((epoch + 1) & 0xFFFFFFFFFFFFull) << 16;
  const unsigned tlo = (unsigned)(tag >> 16) & 0xffffu, thi = (unsigned)(tag >> 32);
  asm volatile("" :: "v"(tq) : "memory");
  TSW(9, 1);
  if (w < 5) {
    mod_chunk(wm0, wm1, cv, part, &ctr[0], modg_all, colA, tag, xi < 28, w, lane, c);
    TS(1);
  }
  unsigned q[4];
  {
    u32x4 wq[20];
#pragma unroll
    for (int j = 0; j < 20; ++j) wq[j] = BLD(rWq, rg * 2 * (NH * DH * 2) + c * 16, ((j >> 1) * 64 + (j & 1)) * (NH * DH * 2), 0);
    CBAR();
    ISSUE(1, i1);
    CBAR();
    if (w == 7) {
      float mean = 0.f;
#pragma unroll
      for (int i = 0; i < 5; ++i) mean += bflo(xv[i]) + bfhi(xv[i]);
      mean = wave_sum(mean) * (1.f / DM);
      float var = 0.f;
#pragma unroll
      for (int i = 0; i < 5; ++i) { const float d0 = bflo(xv[i]) - mean, d1 = bfhi(xv[i]) - mean; var = fmaf(d0, d0, fmaf(d1, d1, var)); }
      var = wave_sum(var) * (1.f / DM);
      const float rstd = __builtin_amdgcn_rsqf(var + 1e-5f);
      WAITV(0);
      TSW(13, 7);
      u32x4 g[10];
      for (unsigned spins = 0;; ++spins) {
#pragma unroll
        for (int i = 0; i < 10; ++i) g[i] = BLD(rs, lane * 16, go + i * 1024, 16);
        CBAR(); SB();
        if (spins == 0) __builtin_amdgcn_s_barrier();
        CBAR(); SB();
        bool ok = true;
#pragma unroll
        for (int i = 0; i < 10; ++i) ok &= (g[i][0] >> 16) == tlo && g[i][1] == thi && (g[i][2] >> 16) == tlo && g[i][3] == thi;
        if (__all(ok) || spins > SPIN_CAP) break;
        CBAR();
      }
      WAITV(0);
#pragma unroll
      for (int i = 0; i < 5; ++i)
        hp[lane + 64 * i] = cvtpk((bflo(xv[i]) - mean) * rstd * (1.f + bflo(g[5 + i][0])) + bflo(g[i][0]),
                                  (bfhi(xv[i]) - mean) * rstd * (1.f + bflo(g[5 + i][2])) + bflo(g[i][2]));
      TSW(2, 7);
    } else {
      __builtin_amdgcn_s_barrier();
      CBAR();
      DMA(reinterpret_cast<unsigned*>(red) + w * 2048, 22 + w);
    }
    CBAR();
    DMA(w1s + w * 2560, 14 + w);
    CBAR();
    ISSUE(2, 45 + w);
    CBAR();
#pragma unroll
    for (int j = 0; j < 10; ++j)
#pragma unroll
      for (int k = 0; k < 4; ++k) PAIRUP(wq[2 * j][k], wq[2 * j + 1][k]);
#pragma unroll
    for (int j = 0; j < 20; j += 2) asm volatile("" : "+v"(wq[j]), "+v"(wq[j + 1]));
    BAR();
    float a[8] = {0.f, 0.f, 0.f, 0.f, 0.f, 0.f, 0.f, 0.f};
#pragma unroll
    for (int j = 0; j < 10; ++j) {
      const unsigned hv = hp[j * 32 + rg];
#pragma unroll
      for (int k = 0; k < 4; ++k) { a[2 * k] = dot2(wq[2 * j][k], hv, a[2 * k]); a[2 * k + 1] = dot2(wq[2 * j + 1][k], hv, a[2 * k + 1]); }
    }
    float o0, o1; fold8(a, o0, o1);
    *reinterpret_cast<f32x2*>(mrg + w * DH + 8 * c + 2 * r) = f32x2{o0, o1};
    BAR();
    float s0 = 0.f, s1 = 0.f;
#pragma unroll
    for (int u = 0; u < NW; ++u) { const f32x2 t = *reinterpret_cast<const f32x2*>(mrg + u * DH + 8 * c + 2 * r); s0 += t[0]; s1 += t[1]; }
    const unsigned pk = cvtpk(s0, s1);
    auto ab = __builtin_amdgcn_permlane16_swap(pk, pk, false, false);
    auto e0 = __builtin_amdgcn_permlane32_swap(ab[0], ab[0], false, false);
    auto e1 = __builtin_amdgcn_permlane32_swap(ab[1], ab[1], false, false);
    q[0] = e0[0]; q[2] = e0[1]; q[1] = e1[0]; q[3] = e1[1];
  }
  TS(3);
  wm0 = BLD(rWm, wmo, colB * 2, 0); wm1 = BLD(rWm, wmo, colB * 2 + NMOD * 2, 0);
  const rsrc_t rW1 = mkrs(W1, DM * FF * 2);
  const rsrc_t rWo = mkrs(Wo + (size_t)h * DH * DM, DH * DM * 2);
  const rsrc_t rW2 = mkrs(W2 + (size_t)fs * 64 * DM, 64 * DM * 2);
  const unsigned to = BLD1(rWo, tid < 320 ? (b * 320 + tid) * 64 : OOB, 0);
  const unsigned t1 = BLD1(rW1, tid >= 320 && tid < 480 ? (80 * b + ((tid - 320) >> 1)) * (FF * 2) + fs * 128 + (tid & 1) * 64 : OOB, 0);
  const unsigned t2 = BLD1(rW2, tid < 160 ? (b * 160 + tid) * 64 : OOB, 0);
  CBAR();
  constexpr float C2 = 0.088388347648318440f * 1.4426950408889634f;
  float m = -3.0e38f, l = 0.f, acc[8] = {0.f, 0.f, 0.f, 0.f, 0.f, 0.f, 0.f, 0.f};
#define PROC(s_) do { float s4_[4]; \
    _Pragma("unroll") for (int t_ = 0; t_ < 4; ++t_) { float d_ = 0.f; \
      _Pragma("unroll") for (int k_ = 0; k_ < 4; ++k_) d_ = dot2(Kr[s_][t_][k_], q[k_], d_); \
      s4_[t_] = row_sum(d_) * C2; } \
    const float mn_ = fmaxf(fmaxf(fmaxf(s4_[0], s4_[1]), fmaxf(s4_[2], s4_[3])), m); \
    const float al_ = __builtin_amdgcn_exp2f(m - mn_); m = mn_; \
    _Pragma("unroll") for (int t_ = 0; t_ < 4; ++t_) s4_[t_] = __builtin_amdgcn_exp2f(s4_[t_] - mn_); \
    l = fmaf(l, al_, (s4_[0] + s4_[1]) + (s4_[2] + s4_[3])); \
    _Pragma("unroll") for (int k_ = 0; k_ < 8; ++k_) acc[k_] *= al_; \
    _Pragma("unroll") for (int t_ = 0; t_ < 4; ++t_) fma8(acc, s4_[t_], Vr[s_][t_]); \
    asm volatile("" : "+v"(acc[0]), "+v"(acc[1]), "+v"(acc[2]), "+v"(acc[3]), "+v"(acc[4]), "+v"(acc[5]), "+v"(acc[6]), "+v"(acc[7]), "+v"(l), "+v"(m) :: "memory"); } while (0)
  unsigned kx = 0;
#define STEP(s_, i_) do { if (i_ < 256u) PROC(s_); SB(); \
    i_ = kx < 25u ? 53u + 8u * kx + w : (w == 7 ? 228u + kx : 256u); ++kx; ISSUE(s_, i_); SB(); } while (0)
#define ROUNDS(n_) for (int it_ = 0; it_ < (n_); ++it_) { STEP(0, i0); STEP(1, i1); }
  if (w < 7) PROC(0);
  SB(); i0 = 29 + w; ISSUE(0, i0); SB();
  if (w < 7) PROC(1);
  SB(); i1 = 37 + w; ISSUE(1, i1); SB();
  CBAR();
  if (w < 7) { FROMLDS(3, reinterpret_cast<const unsigned*>(red) + w * 2048); PROC(3); SB(); }
  FROMLDS(3, w1s + w * 2560); PROC(3); SB();
  PROC(2); SB();
  CBAR();
  if (w < 5) mod_chunk(wm0, wm1, cv, part + 320, &ctr[1], modg_all, colB, tag, true, w, lane, c);
  asm volatile("" :: "v"(to), "v"(t1), "v"(t2) : "memory");
  {
    u32x4 t[10];
#pragma unroll
    for (int i = 0; i < 10; ++i) t[i] = BLD(rW1, (lane >> 3) * (2 * FF * 2) + (lane & 7) * 16, (w * 80 + (i >> 1) * 16 + (i & 1)) * (FF * 2) + fs * 128, 0);
    CBAR();
    ROUNDS(1);
    CBAR();
#pragma unroll
    for (int i = 0; i < 5; ++i) {
      const int pr = w * 40 + i * 8 + (lane >> 3);
#pragma unroll
      for (int k = 0; k < 4; ++k) PAIRUP(t[2 * i][k], t[2 * i + 1][k]);
      *reinterpret_cast<u32x4*>(w1s + pr * 64 + (((2 * (lane & 7)) ^ (pr & 15)) * 4)) = u32x4{t[2 * i][0], t[2 * i + 1][0], t[2 * i][1], t[2 * i + 1][1]};
      *reinterpret_cast<u32x4*>(w1s + pr * 64 + (((2 * (lane & 7) + 1) ^ (pr & 15)) * 4)) = u32x4{t[2 * i][2], t[2 * i + 1][2], t[2 * i][3], t[2 * i + 1][3]};
    }
  }
  CBAR();
  const int hf = lane >> 5, l32 = lane & 31;
  u32x2 wo[40], w2[20];
#pragma unroll
  for (int i = 0; i < 8; ++i)
#pragma unroll
    for (int jb = 0; jb < 5; ++jb) wo[i * 5 + jb] = BLD2(rWo, hf * 8 * (DM * 2) + l32 * 8, (16 * w + i) * (DM * 2) + jb * 256);
  CBAR();
  ROUNDS(1);
  CBAR();
#pragma unroll
  for (int i = 0; i < 4; ++i)
#pragma unroll
    for (int jb = 0; jb < 5; ++jb) w2[i * 5 + jb] = BLD2(rW2, hf * 4 * (DM * 2) + l32 * 8, (8 * w + i) * (DM * 2) + jb * 256);
  CBAR();
#pragma unroll
  for (int i = 0; i < 8; i += 2)
#pragma unroll
    for (int jb = 0; jb < 5; ++jb) { PAIRUP(wo[i * 5 + jb][0], wo[i * 5 + 5 + jb][0]); PAIRUP(wo[i * 5 + jb][1], wo[i * 5 + 5 + jb][1]);
      asm volatile("" : "+v"(wo[i * 5 + jb]), "+v"(wo[i * 5 + 5 + jb])); }
  ROUNDS(2);
  CBAR();
#pragma unroll
  for (int i = 0; i < 4; i += 2)
#pragma unroll
    for (int jb = 0; jb < 5; ++jb) { PAIRUP(w2[i * 5 + jb][0], w2[i * 5 + 5 + jb][0]); PAIRUP(w2[i * 5 + jb][1], w2[i * 5 + 5 + jb][1]);
      asm volatile("" : "+v"(w2[i * 5 + jb]), "+v"(w2[i * 5 + 5 + jb])); }
  {
    u32x4 g[3];
    for (unsigned spins = 0;; ++spins) {
#pragma unroll
      for (int j = 0; j < 3; ++j) g[j] = BLD(rs, (j < 2 || tid < 256 ? tid : 0) * 16, go + 2 * DM * 8 + j * NT * 16, 16);
      CBAR();
      ROUNDS(1);
      bool ok = true;
#pragma unroll
      for (int j = 0; j < 3; ++j) ok &= (g[j][0] >> 16) == tlo && g[j][1] == thi && (g[j][2] >> 16) == tlo && g[j][3] == thi;
      if (__all(ok) || spins > SPIN_CAP) break;
    }
#pragma unroll
    for (int j = 0; j < 3; ++j) if (j < 2 || tid < 256) {
      const int k = 2 * (tid + NT * j);
      const float add = (k >= 2 * DM && k < 3 * DM) ? 1.f : 0.f;
      *reinterpret_cast<f32x2*>(modr + k) = f32x2{bflo(g[j][0]) + add, bflo(g[j][2]) + add};
    }
  }
  CBAR();
  do { STEP(0, i0); STEP(1, i1); } while (i0 < 256u);
  CBAR();
  TS(4);
  {
    const float M = xrow_max(m), f = __builtin_amdgcn_exp2f(m - M);
    l = xrow_sum(l * f);
#pragma unroll
    for (int k = 0; k < 8; ++k) acc[k] *= f;
    float o0, o1; fold8(acc, o0, o1);
    *reinterpret_cast<f32x2*>(mrg + w * (DH + 4) + 4 + 8 * c + 2 * r) = f32x2{o0, o1};
    if (lane == 0) *reinterpret_cast<f32x2*>(mrg + w * (DH + 4)) = f32x2{M, l};
  }
  BAR();
  TSW(10, 0);
  {
    f32x2 ml[NW]; float av[NW];
#pragma unroll
    for (int u = 0; u < NW; ++u) { ml[u] = *reinterpret_cast<const f32x2*>(mrg + u * (DH + 4)); av[u] = mrg[u * (DH + 4) + 4 + 16 * w + c]; }
    float M = ml[0][0];
#pragma unroll
    for (int u = 1; u < NW; ++u) M = fmaxf(M, ml[u][0]);
    float L = 0.f, ov = 0.f;
#pragma unroll
    for (int u = 0; u < NW; ++u) { const float f = __builtin_amdgcn_exp2f(ml[u][0] - M); L = fmaf(f, ml[u][1], L); ov = fmaf(f, av[u], ov); }
    if (lane < 16) ob[16 * w + c] = (u16)cvtpk(ov * __builtin_amdgcn_rcpf(L), 0.f);
    LDSWAIT(); CBAR();
    const u32x4 op = *reinterpret_cast<const u32x4*>(ob + 16 * w + 8 * hf);
#pragma unroll
    for (int jb = 0; jb < 5; ++jb) {
      float a[4] = {0.f, 0.f, 0.f, 0.f};
#pragma unroll
      for (int p = 0; p < 4; ++p) {
        a[0] = dot2(wo[p * 10 + jb][0], op[p], a[0]); a[1] = dot2(wo[p * 10 + 5 + jb][0], op[p], a[1]);
        a[2] = dot2(wo[p * 10 + jb][1], op[p], a[2]); a[3] = dot2(wo[p * 10 + 5 + jb][1], op[p], a[3]);
      }
      *reinterpret_cast<f32x2*>(red + w * DM + jb * 128 + 4 * l32 + 2 * hf) = f32x2{fold32(a[0], a[2]), fold32(a[1], a[3])};
    }
  }
  BAR();
  if (tid < DM / 4) {
    f32x4 s = *reinterpret_cast<const f32x4*>(red + tid * 4);
#pragma unroll
    for (int u = 1; u < NW; ++u) s += *reinterpret_cast<const f32x4*>(red + u * DM + tid * 4);
    const u32x4 o = {nosent(s[0]), nosent(s[1]), nosent(s[2]), nosent(s[3])};
    __builtin_amdgcn_raw_buffer_store_b128(o, rs, p1off + h * DM * 4 + tid * 16, 0, 16);
  }
  CBAR();
  TS(5);
  float* red2 = red + NW * DM;
  f32x2 sb[5], cb[5];
  u32x4 wa[5], wb[5];
#pragma unroll
  for (int i = 0; i < 5; ++i) {
    sb[i] = *reinterpret_cast<const f32x2*>(modr + DM + 2 * lane + 128 * i); cb[i] = *reinterpret_cast<const f32x2*>(modr + 2 * DM + 2 * lane + 128 * i);
    wa[i] = *reinterpret_cast<const u32x4*>(w1s + (lane + 64 * i) * 64 + (((2 * w) ^ c) * 4));
    wb[i] = *reinterpret_cast<const u32x4*>(w1s + (lane + 64 * i) * 64 + (((2 * w + 1) ^ c) * 4));
  }
  const f32x2 ga = *reinterpret_cast<const f32x2*>(modr + (tid < DM / 2 ? tid * 2 : 0));
  CBAR();
  {
    f32x4 s[3];
    const int c2 = lane + 128 < DM / 4 ? lane + 128 : lane;
    for (unsigned spins = 0;; ++spins) {
      u32x4 g[12];
#pragma unroll
      for (int p = 0; p < 4; ++p) {
        const unsigned base = p1off + (4 * w + p) * DM * 4;
        g[p * 3 + 0] = BLD(rs, lane * 16, base, 16);
        g[p * 3 + 1] = BLD(rs, lane * 16, base + 1024, 16);
        g[p * 3 + 2] = BLD(rs, c2 * 16, base, 16);
      }
      unsigned mn = 0;
#pragma unroll
      for (int k = 0; k < 12; ++k) mn = max(max(mn, max(g[k][0], g[k][1])), max(g[k][2], g[k][3]));
#pragma unroll
      for (int k = 0; k < 3; ++k)
        s[k] = (__builtin_bit_cast(f32x4, g[k]) + __builtin_bit_cast(f32x4, g[3 + k])) + (__builtin_bit_cast(f32x4, g[6 + k]) + __builtin_bit_cast(f32x4, g[9 + k]));
      if (__all(mn != SENT) || spins > SPIN_CAP) break;
      __builtin_amdgcn_s_sleep(PSL);
      CBAR();
    }
    *reinterpret_cast<f32x4*>(red2 + w * DM + lane * 4) = s[0];
    *reinterpret_cast<f32x4*>(red2 + w * DM + (lane + 64) * 4) = s[1];
    if (lane < 32) *reinterpret_cast<f32x4*>(red2 + w * DM + (lane + 128) * 4) = s[2];
  }
  TS(6);
  BAR();
  if (tid < DM / 2) {
    f32x2 s = *reinterpret_cast<const f32x2*>(red2 + tid * 2);
#pragma unroll
    for (int u = 1; u < NW; ++u) s += *reinterpret_cast<const f32x2*>(red2 + u * DM + tid * 2);
    const unsigned sb2 = cvtpk(s[0], s[1]);
    const unsigned pr2 = cvtpk(ga[0] * bflo(sb2), ga[1] * bfhi(sb2));
    const unsigned x2 = cvtpk(bflo(xa) + bflo(pr2), bfhi(xa) + bfhi(pr2));
    *reinterpret_cast<f32x2*>(vec + tid * 2) = f32x2{bflo(x2), bfhi(x2)};
  }
  BAR();
  if (h < NFW) {
    f32x2 x1[5];
#pragma unroll
    for (int i = 0; i < 5; ++i) x1[i] = *reinterpret_cast<const f32x2*>(vec + 2 * lane + 128 * i);
    float mu = 0.f;
#pragma unroll
    for (int i = 0; i < 5; ++i) mu += x1[i][0] + x1[i][1];
    mu = wave_sum(mu) * (1.f / DM);
    float vr = 0.f;
#pragma unroll
    for (int i = 0; i < 5; ++i) { x1[i] -= mu; vr = fmaf(x1[i][0], x1[i][0], fmaf(x1[i][1], x1[i][1], vr)); }
    vr = wave_sum(vr) * (1.f / DM);
    const float rs2 = __builtin_amdgcn_rsqf(vr + 1e-5f);
    float a[8] = {0.f, 0.f, 0.f, 0.f, 0.f, 0.f, 0.f, 0.f};
#pragma unroll
    for (int i = 0; i < 5; ++i) {
      const unsigned h2 = cvtpk(fmaf(x1[i][0] * rs2, cb[i][0], sb[i][0]), fmaf(x1[i][1] * rs2, cb[i][1], sb[i][1]));
#pragma unroll
      for (int k = 0; k < 4; ++k) { a[k] = dot2(wa[i][k], h2, a[k]); a[4 + k] = dot2(wb[i][k], h2, a[4 + k]); }
    }
    float o0, o1; fold8(a, o0, o1); o0 = silu(rbf(row_sum(o0))); o1 = silu(rbf(row_sum(o1)));
    const unsigned pk = cvtpk(o0, o1);
    auto ab = __builtin_amdgcn_permlane16_swap(pk, pk, false, false);
#pragma unroll
    for (int jb = 0; jb < 5; ++jb) {
      float a2[4] = {0.f, 0.f, 0.f, 0.f};
#pragma unroll
      for (int p = 0; p < 2; ++p) {
        a2[0] = dot2(w2[p * 10 + jb][0], ab[p], a2[0]); a2[1] = dot2(w2[p * 10 + 5 + jb][0], ab[p], a2[1]);
        a2[2] = dot2(w2[p * 10 + jb][1], ab[p], a2[2]); a2[3] = dot2(w2[p * 10 + 5 + jb][1], ab[p], a2[3]);
      }
      *reinterpret_cast<f32x2*>(red + w * DM + jb * 128 + 4 * l32 + 2 * hf) = f32x2{fold32(a2[0], a2[2]), fold32(a2[1], a2[3])};
    }
    BAR();
    if (tid < DM / 4) {
      f32x4 s = *reinterpret_cast<const f32x4*>(red + tid * 4);
#pragma unroll
      for (int u = 1; u < NW; ++u) s += *reinterpret_cast<const f32x4*>(red + u * DM + tid * 4);
      const u32x4 o = {nosent(s[0]), nosent(s[1]), nosent(s[2]), nosent(s[3])};
      __builtin_amdgcn_raw_buffer_store_b128(o, rs, p2off + h * DM * 4 + tid * 16, 0, 16);
    }
    CBAR();
  }
  TS(7);
  if (w == 3) {
    const int ch = lane % 5, pg = lane / 5;
    f32x4 s;
    for (unsigned spins = 0;; ++spins) {
      u32x4 g[3];
      bool ok = true;
      s = f32x4{0.f, 0.f, 0.f, 0.f};
#pragma unroll
      for (int k = 0; k < 3; ++k) {
        const int p = pg + 12 * k; const bool live = lane < 60 && p < NFW;
        g[k] = __builtin_amdgcn_raw_buffer_load_b128(rs, p2off + (live ? p : 0) * DM * 4 + (h * 5 + ch) * 16, 0, 16);
      }
#pragma unroll
      for (int k = 0; k < 3; ++k) {
        const int p = pg + 12 * k; const bool live = lane < 60 && p < NFW;
        ok &= g[k][0] != SENT && g[k][1] != SENT && g[k][2] != SENT && g[k][3] != SENT;
        if (live) s += __builtin_bit_cast(f32x4, g[k]);
      }
      if (__all(ok) || spins > SPIN_CAP) break;
      __builtin_amdgcn_s_sleep(2);
      CBAR();
    }
    *reinterpret_cast<f32x4*>(mrg + lane * 4) = s;
    __builtin_amdgcn_s_waitcnt(0xC07F);
    CBAR();
    if (lane < 20) {
      float t = 0.f;
#pragma unroll
      for (int u = 0; u < 12; ++u) t += mrg[u * 20 + lane];
      const int col = h * 20 + lane;
      const float o = vec[col] + rbf(modr[3 * DM + col] * rbf(t));
      Out[b * DM + col] = (u16)(cvtpk(o, 0.f) & 0xffffu);
    }
    if (bid == 0 && lane == 0) stg(reinterpret_cast<u64*>(ws + WS_EPOCH), epoch + 1);
    TSW(8, 3); TSR(17, 3);
  }
}

extern "C" void kernel_launch(void* const* d_in, const int* in_sizes, int n_in, void* d_out, int out_size, void* d_ws, size_t ws_size,
                              hipStream_t stream) {
  (void)in_sizes; (void)n_in; (void)out_size; (void)ws_size;
  hipLaunchKernelGGL(fused_block, dim3(NB * NH), dim3(NT), 0, stream, (const u16*)d_in[0], (const u16*)d_in[1], (const u16*)d_in[2],
                     (const u16*)d_in[3], (const u16*)d_in[4], (const u16*)d_in[6], (const u16*)d_in[7], (const u16*)d_in[8],
                     (const u16*)d_in[9], (u16*)d_out, (unsigned char*)d_ws);
}
```

```cpp
#include <hip/hip_runtime.h>
#include <hip/hip_bf16.h>
#include <stdint.h>

typedef unsigned short u16;
typedef unsigned long long u64;
using u32x4 = __attribute__((ext_vector_type(4))) unsigned;
using u32x2 = __attribute__((ext_vector_type(2))) unsigned;
using f32x4 = __attribute__((ext_vector_type(4))) float;
using f32x2 = __attribute__((ext_vector_type(2))) float;
using bf16x2 = __attribute__((ext_vector_type(2))) __bf16;
typedef __attribute__((address_space(1))) u64 gu64;

constexpr int NB = 8, DM = 640, NH = 32, SK = 4096, DH = 128, FF = 1728, NMOD = 6 * DM;
constexpr int NT = 512, NW = 8;
constexpr int NFW = FF / 64;
constexpr size_t WS_EPOCH = 0;
constexpr size_t WS_MOD = 4096;
constexpr size_t WS_P1 = WS_MOD + (size_t)NB * NMOD * 8;
constexpr size_t WS_P2 = WS_P1 + (size_t)NB * NH * DM * 4;
constexpr size_t WS_END = WS_P2 + (size_t)NB * NH * DM * 4;
constexpr size_t WS_DBG = 4u << 20;
constexpr unsigned SENT = 0xFFFFFFFFu;
constexpr unsigned SPIN_CAP = 1u << 22;

#define KVAUX 2

__device__ __forceinline__ float bf2f(unsigned v) { return __builtin_bit_cast(float, v << 16); }
__device__ __forceinline__ float bflo(unsigned v) { return __builtin_bit_cast(float, v << 16); }
__device__ __forceinline__ float bfhi(unsigned v) { return __builtin_bit_cast(float, v & 0xffff0000u); }
__device__ __forceinline__ unsigned cvtpk(float lo, float hi) {
  unsigned r; asm("v_cvt_pk_bf16_f32 %0, %1, %2" : "=v"(r) : "v"(lo), "v"(hi)); return r;
}
__device__ __forceinline__ float rbf(float x) { return bflo(cvtpk(x, x)); }
__device__ __forceinline__ float dot2(unsigned k, unsigned q, float acc) {
  return __builtin_amdgcn_fdot2_f32_bf16(__builtin_bit_cast(bf16x2, k), __builtin_bit_cast(bf16x2, q), acc, false);
}
template <int CTRL> __device__ __forceinline__ float dpp(float x) {
  return __builtin_bit_cast(float, __builtin_amdgcn_mov_dpp(__builtin_bit_cast(int, x), CTRL, 0xf, 0xf, true));
}
__device__ __forceinline__ float row_sum(float x) {
  x += dpp<0x128>(x); x += dpp<0x124>(x); x += dpp<0x122>(x); x += dpp<0x121>(x); return x;
}
__device__ __forceinline__ float fold32(float a, float b) {
  auto s = __builtin_amdgcn_permlane32_swap(__float_as_uint(a), __float_as_uint(b), false, false);
  return __uint_as_float(s[0]) + __uint_as_float(s[1]);
}
__device__ __forceinline__ float fold16(float a, float b) {
  auto s = __builtin_amdgcn_permlane16_swap(__float_as_uint(a), __float_as_uint(b), false, false);
  return __uint_as_float(s[0]) + __uint_as_float(s[1]);
}
__device__ __forceinline__ float xrow_sum(float x) { x = fold16(x, x); return fold32(x, x); }
__device__ __forceinline__ float xrow_max(float x) {
  auto s = __builtin_amdgcn_permlane16_swap(__float_as_uint(x), __float_as_uint(x), false, false);
  x = fmaxf(__uint_as_float(s[0]), __uint_as_float(s[1]));
  auto t = __builtin_amdgcn_permlane32_swap(__float_as_uint(x), __float_as_uint(x), false, false);
  return fmaxf(__uint_as_float(t[0]), __uint_as_float(t[1]));
}
__device__ __forceinline__ float wave_sum(float x) { return xrow_sum(row_sum(x)); }
typedef __amdgpu_buffer_rsrc_t rsrc_t;
typedef __attribute__((address_space(3))) void* lds_ptr;
__device__ __forceinline__ rsrc_t mkrs(const void* p, unsigned bytes) { return __builtin_amdgcn_make_buffer_rsrc((void*)p, 0, (int)bytes, 0x00020000); }
#define BLD(rs_, vo_, so_, aux_) __builtin_amdgcn_raw_buffer_load_b128(rs_, vo_, so_, aux_)
__device__ __forceinline__ u64 ldg(const u64* p) { return __hip_atomic_load((gu64*)p, __ATOMIC_RELAXED, __HIP_MEMORY_SCOPE_AGENT); }
__device__ __forceinline__ void stg(u64* p, u64 v) { __hip_atomic_store((gu64*)p, v, __ATOMIC_RELAXED, __HIP_MEMORY_SCOPE_AGENT); }
__device__ __forceinline__ float silu(float x) { return x * __builtin_amdgcn_rcpf(1.f + __builtin_amdgcn_exp2f(-1.4426950408889634f * x)); }
#define CBAR() asm volatile("" ::: "memory")
#define SB() __builtin_amdgcn_sched_barrier(0)
__device__ __forceinline__ void fma8(float* a, float s, u32x4 w) {
#pragma unroll
  for (int k = 0; k < 4; ++k) { a[2 * k] = fmaf(s, bflo(w[k]), a[2 * k]); a[2 * k + 1] = fmaf(s, bfhi(w[k]), a[2 * k + 1]); }
}
__device__ __forceinline__ void fold8(const float* a, float& o0, float& o1) {
  const float u0 = fold32(a[0], a[4]), u1 = fold32(a[1], a[5]), u2 = fold32(a[2], a[6]), u3 = fold32(a[3], a[7]);
  o0 = fold16(u0, u2); o1 = fold16(u1, u3);
}
__device__ __forceinline__ unsigned nosent(float v) { const unsigned u = __float_as_uint(v); return u == SENT ? 0x7FC00000u : u; }

#define TSW(i, wv) do {} while (0)
#define TSR(i, wv) do {} while (0)
#define TS(i) TSW(i, 0)

#define BLD2(rs_, vo_, so_) __builtin_amdgcn_raw_buffer_load_b64(rs_, vo_, so_, 0)
#define BLD1(rs_, vo_, so_) __builtin_amdgcn_raw_buffer_load_b32(rs_, vo_, so_, 0)
constexpr int OOB = 0x7fffff00;
#define PAIRUP(a_, b_) do { const unsigned x_ = (a_), y_ = (b_); a_ = __builtin_amdgcn_perm(y_, x_, 0x05040100u); b_ = __builtin_amdgcn_perm(y_, x_, 0x07060302u); } while (0)
#define WAITV(n) asm volatile("s_waitcnt vmcnt(" #n ")" ::: "memory")
#define LDSWAIT() __builtin_amdgcn_s_waitcnt(0xC07F)
#define BAR() do { LDSWAIT(); __builtin_amdgcn_s_barrier(); CBAR(); } while (0)
#define TRS(HALF, CTRL) _Pragma("unroll") for (int i_ = 0; i_ < (HALF); ++i_) { const float lo_ = t[i_], hi_ = t[i_ + (HALF)]; \
    const float a_ = lo_ + dpp<CTRL>(lo_), b_ = hi_ + dpp<CTRL>(hi_); t[i_] = (c & (HALF)) ? b_ : a_; }

__device__ __forceinline__ void mod_chunk(const u32x4 w0, const u32x4 w1, const unsigned* cv, float* part, unsigned* cnt, u64* modg_all, int col, u64 tag,
                                          bool publish, int w, int lane, int c) {
  float u[32];
  float wa[8], wb[8];
#pragma unroll
  for (int k = 0; k < 4; ++k) { wa[2 * k] = bflo(w0[k]); wa[2 * k + 1] = bfhi(w0[k]); wb[2 * k] = bflo(w1[k]); wb[2 * k + 1] = bfhi(w1[k]); }
#pragma unroll
  for (int bb = 0; bb < 4; ++bb) {
    const float s0 = rbf(silu(bflo(cv[bb]))), s1 = rbf(silu(bfhi(cv[bb]))), z0 = rbf(silu(bflo(cv[bb + 4]))), z1 = rbf(silu(bfhi(cv[bb + 4])));
#pragma unroll
    for (int e = 0; e < 8; ++e) u[bb * 8 + e] = fold32(fmaf(s0, wa[e], s1 * wb[e]), fmaf(z0, wa[e], z1 * wb[e]));
  }
  float t[16];
#pragma unroll
  for (int i = 0; i < 16; ++i) t[i] = fold16(u[i], u[i + 16]);
  TRS(8, 0x128) TRS(4, 0x141) TRS(2, 0x4E) TRS(1, 0xB1)
  part[w * 64 + lane] = t[0];
  LDSWAIT();
  unsigned old = 0;
  if (lane == 0) old = __hip_atomic_fetch_add(cnt, 1u, __ATOMIC_RELAXED, __HIP_MEMORY_SCOPE_WORKGROUP);
  old = __builtin_amdgcn_readfirstlane(old);
  CBAR();
  if (old == 4u && publish) {
    const float v = ((part[lane] + part[64 + lane]) + (part[128 + lane] + part[192 + lane])) + part[256 + lane];
    stg(modg_all + (size_t)(lane >> 3) * NMOD + col + (lane & 7), tag | (u64)(cvtpk(v, 0.f) & 0xffffu));
  }
  CBAR();
}

__global__ void __launch_bounds__(NT, 2)
fused_block(const u16* X, const u16* Kc, const u16* Vc, const u16* Wq, const u16* Wo, const u16* W1, const u16* W2, const u16* Cc,
            const u16* Wm, u16* Out, unsigned char* ws) {
  __shared__ __attribute__((aligned(16))) unsigned w1s[DM * 32];
  __shared__ __attribute__((aligned(16))) float red[7 * 2048];
  __shared__ __attribute__((aligned(16))) float vec[DM];
  __shared__ __attribute__((aligned(16))) float modr[4 * DM];
  __shared__ __attribute__((aligned(16))) float mrg[NW * (DH + 4)];
  __shared__ __attribute__((aligned(16))) float part[2 * 320];
  __shared__ __attribute__((aligned(16))) unsigned hp[DM / 2];
  __shared__ __attribute__((aligned(16))) u16 ob[DH];
  __shared__ unsigned ctr[2];
  const int tid = threadIdx.x, lane = tid & 63, w = __builtin_amdgcn_readfirstlane(tid >> 6), r = lane >> 4, c = lane & 15;
  const int bid = blockIdx.x, b = bid >> 5, h = bid & 31;
  TSW(0, 6); TSR(16, 6);
  u64* modg_all = reinterpret_cast<u64*>(ws + WS_MOD);
  const rsrc_t rs = mkrs(ws, (unsigned)WS_END);
  const unsigned p1off = (unsigned)WS_P1 + (unsigned)b * NH * DM * 4, p2off = (unsigned)WS_P2 + (unsigned)b * NH * DM * 4;
  const unsigned go = (unsigned)WS_MOD + (unsigned)b * NMOD * 8;
  if (w == 7) {
    const u32x4 sv = {SENT, SENT, SENT, SENT};
#pragma unroll
    for (int j = 0; j < 3; ++j) if (lane + 64 * j < DM / 4) {
      __builtin_amdgcn_raw_buffer_store_b128(sv, rs, p1off + h * DM * 4 + (lane + 64 * j) * 16, 0, 16);
      __builtin_amdgcn_raw_buffer_store_b128(sv, rs, p2off + h * DM * 4 + (lane + 64 * j) * 16, 0, 16);
    }
    if (lane < 2) ctr[lane] = 0u;
  }
  CBAR();
  const u64 epoch = *(const __attribute__((address_space(4))) u64*)(ws + WS_EPOCH);
  const int xs = bid & 7, xi = bid >> 3;
  const int colA = 8 * (xi < 20 ? xs * 20 + xi : 416 + xs * 8 + ((xi - 20) & 7));
  const int colB = 8 * (160 + xs * 32 + xi);
  const rsrc_t rWm = mkrs(Wm, DM * NMOD * 2);
  const rsrc_t rC = mkrs(Cc, NB * DM * 2);
  const rsrc_t rX = mkrs(X + b * DM, DM * 2);
  const int wmo = w < 5 ? (w * 128 + 2 * lane) * (NMOD * 2) : OOB;
  u32x4 wm0 = BLD(rWm, wmo, colA * 2, 0), wm1 = BLD(rWm, wmo, colA * 2 + NMOD * 2, 0);
  unsigned cv[8];
#pragma unroll
  for (int i = 0; i < 8; ++i) cv[i] = BLD1(rC, w < 5 ? w * 256 + lane * 4 : OOB, i * DM * 2);
  unsigned xv[5];
#pragma unroll
  for (int i = 0; i < 5; ++i) xv[i] = BLD1(rX, w == 7 ? lane * 4 : OOB, i * 256);
  const unsigned xa = BLD1(rX, tid * 4, 0);
  const rsrc_t rWq = mkrs(Wq + h * DH, (DM * NH * DH - h * DH) * 2);
  const unsigned tq = BLD1(rWq, tid < 320 ? (80 * b + (tid >> 2)) * (NH * DH * 2) + (tid & 3) * 64 : OOB, 0);
  BAR();
  u32x4 Kr[4][4], Vr[4][4];
  const rsrc_t rK = mkrs(Kc + (size_t)bid * SK * DH, SK * DH * 2), rV = mkrs(Vc + (size_t)bid * SK * DH, SK * DH * 2);
  const int kvo = lane * 16;
#define ISSUE(s_, i_) do { const unsigned ti_ = (unsigned)(i_) * (16 * DH * 2); \
    _Pragma("unroll") for (int t_ = 0; t_ < 4; ++t_) { Kr[s_][t_] = BLD(rK, kvo, ti_ + t_ * 1024, KVAUX); Vr[s_][t_] = BLD(rV, kvo, ti_ + t_ * 1024, KVAUX); } } while (0)
#define DMA1(rs_, dst_, o_) __builtin_amdgcn_raw_ptr_buffer_load_lds(rs_, (lds_ptr)(dst_), 16, kvo, ti_, o_, KVAUX)
#define DMA(dst_, i_) do { const unsigned ti_ = (unsigned)(i_) * (16 * DH * 2); \
    DMA1(rK, dst_, 0); DMA1(rK, dst_, 1024); DMA1(rK, dst_, 2048); DMA1(rK, dst_, 3072); \
    DMA1(rV, (dst_) + 1024, 0); DMA1(rV, (dst_) + 1024, 1024); DMA1(rV, (dst_) + 1024, 2048); DMA1(rV, (dst_) + 1024, 3072); } while (0)
#define FROMLDS(s_, src_) do { _Pragma("unroll") for (int t_ = 0; t_ < 4; ++t_) { Kr[s_][t_] = *reinterpret_cast<const u32x4*>((src_) + t_ * 256 + lane * 4); \
    Vr[s_][t_] = *reinterpret_cast<const u32x4*>((src_) + 1024 + t_ * 256 + lane * 4); } } while (0)
  const int fs = h < NFW ? h : 0;
  const int rg = w * 4 + r;
  unsigned i0 = w < 7 ? w : 4096u, i1 = w < 7 ? 7 + w : 4096u;
  ISSUE(0, i0);
  CBAR();
  const u64 tag = ((epoch + 1) & 0xFFFFFFFFFFFFull) << 16;
  const unsigned tlo = (unsigned)(tag >> 16) & 0xffffu, thi = (unsigned)(tag >> 32);
  asm volatile("" :: "v"(tq) : "memory");
  TSW(9, 1);
  if (w < 5) {
    mod_chunk(wm0, wm1, cv, part, &ctr[0], modg_all, colA, tag, xi < 28, w, lane, c);
    TS(1);
  }
  __builtin_amdgcn_s_barrier();
  CBAR();
  unsigned q[4];
  {
    u32x4 wq[20];
#pragma unroll
    for (int j = 0; j < 20; ++j) wq[j] = BLD(rWq, rg * 2 * (NH * DH * 2) + c * 16, ((j >> 1) * 64 + (j & 1)) * (NH * DH * 2), 0);
    CBAR();
    ISSUE(1, i1);
    CBAR();
    if (w == 7) {
      float mean = 0.f;
#pragma unroll
      for (int i = 0; i < 5; ++i) mean += bflo(xv[i]) + bfhi(xv[i]);
      mean = wave_sum(mean) * (1.f / DM);
      float var = 0.f;
#pragma unroll
      for (int i = 0; i < 5; ++i) { const float d0 = bflo(xv[i]) - mean, d1 = bfhi(xv[i]) - mean; var = fmaf(d0, d0, fmaf(d1, d1, var)); }
      var = wave_sum(var) * (1.f / DM);
      const float rstd = __builtin_amdgcn_rsqf(var + 1e-5f);
      WAITV(0);
      TSW(13, 7);
      u32x4 g[10];
      for (unsigned spins = 0;; ++spins) {
#pragma unroll
        for (int i = 0; i < 10; ++i) g[i] = BLD(rs, lane * 16, go + i * 1024, 16);
        CBAR(); SB();
        if (spins == 0) __builtin_amdgcn_s_barrier();
        CBAR(); SB();
        bool ok = true;
#pragma unroll
        for (int i = 0; i < 10; ++i) ok &= (g[i][0] >> 16) == tlo && g[i][1] == thi && (g[i][2] >> 16) == tlo && g[i][3] == thi;
        if (__all(ok) || spins > SPIN_CAP) break;
        CBAR();
      }
      WAITV(0);
#pragma unroll
      for (int i = 0; i < 5; ++i)
        hp[lane + 64 * i] = cvtpk((bflo(xv[i]) - mean) * rstd * (1.f + bflo(g[5 + i][0])) + bflo(g[i][0]),
                                  (bfhi(xv[i]) - mean) * rstd * (1.f + bflo(g[5 + i][2])) + bflo(g[i][2]));
      TSW(2, 7);
    } else {
      DMA(reinterpret_cast<unsigned*>(red) + w * 2048, 22 + w);
      CBAR();
      __builtin_amdgcn_s_barrier();
      CBAR();
    }
    CBAR();
    DMA(w1s + w * 2560, 14 + w);
    CBAR();
    ISSUE(2, 45 + w);
    CBAR();
#pragma unroll
    for (int j = 0; j < 10; ++j)
#pragma unroll
      for (int k = 0; k < 4; ++k) PAIRUP(wq[2 * j][k], wq[2 * j + 1][k]);
#pragma unroll
    for (int j = 0; j < 20; j += 2) asm volatile("" : "+v"(wq[j]), "+v"(wq[j + 1]));
    BAR();
    float a[8] = {0.f, 0.f, 0.f, 0.f, 0.f, 0.f, 0.f, 0.f};
#pragma unroll
    for (int j = 0; j < 10; ++j) {
      const unsigned hv = hp[j * 32 + rg];
#pragma unroll
      for (int k = 0; k < 4; ++k) { a[2 * k] = dot2(wq[2 * j][k], hv, a[2 * k]); a[2 * k + 1] = dot2(wq[2 * j + 1][k], hv, a[2 * k + 1]); }
    }
    float o0, o1; fold8(a, o0, o1);
    *reinterpret_cast<f32x2*>(mrg + w * DH + 8 * c + 2 * r) = f32x2{o0, o1};
    BAR();
    float s0 = 0.f, s1 = 0.f;
#pragma unroll
    for (int u = 0; u < NW; ++u) { const f32x2 t = *reinterpret_cast<const f32x2*>(mrg + u * DH + 8 * c + 2 * r); s0 += t[0]; s1 += t[1]; }
    const unsigned pk = cvtpk(s0, s1);
    auto ab = __builtin_amdgcn_permlane16_swap(pk, pk, false, false);
    auto e0 = __builtin_amdgcn_permlane32_swap(ab[0], ab[0], false, false);
    auto e1 = __builtin_amdgcn_permlane32_swap(ab[1], ab[1], false, false);
    q[0] = e0[0]; q[2] = e0[1]; q[1] = e1[0]; q[3] = e1[1];
  }
  TS(3);
  wm0 = BLD(rWm, wmo, colB * 2, 0); wm1 = BLD(rWm, wmo, colB * 2 + NMOD * 2, 0);
  const rsrc_t rW1 = mkrs(W1, DM * FF * 2);
  const rsrc_t rWo = mkrs(Wo + (size_t)h * DH * DM, DH * DM * 2);
  const rsrc_t rW2 = mkrs(W2 + (size_t)fs * 64 * DM, 64 * DM * 2);
  const unsigned to = BLD1(rWo, tid < 320 ? (b * 320 + tid) * 64 : OOB, 0);
  const unsigned t1 = BLD1(rW1, tid >= 320 && tid < 480 ? (80 * b + ((tid - 320) >> 1)) * (FF * 2) + fs * 128 + (tid & 1) * 64 : OOB, 0);
  const unsigned t2 = BLD1(rW2, tid < 160 ? (b * 160 + tid) * 64 : OOB, 0);
  CBAR();
  constexpr float C2 = 0.088388347648318440f * 1.4426950408889634f;
  float m = -3.0e38f, l = 0.f, acc[8] = {0.f, 0.f, 0.f, 0.f, 0.f, 0.f, 0.f, 0.f};
#define PROC(s_) do { float s4_[4]; \
    _Pragma("unroll") for (int t_ = 0; t_ < 4; ++t_) { float d_ = 0.f; \
      _Pragma("unroll") for (int k_ = 0; k_ < 4; ++k_) d_ = dot2(Kr[s_][t_][k_], q[k_], d_); \
      s4_[t_] = row_sum(d_) * C2; } \
    const float mn_ = fmaxf(fmaxf(fmaxf(s4_[0], s4_[1]), fmaxf(s4_[2], s4_[3])), m); \
    const float al_ = __builtin_amdgcn_exp2f(m - mn_); m = mn_; \
    _Pragma("unroll") for (int t_ = 0; t_ < 4; ++t_) s4_[t_] = __builtin_amdgcn_exp2f(s4_[t_] - mn_); \
    l = fmaf(l, al_, (s4_[0] + s4_[1]) + (s4_[2] + s4_[3])); \
    _Pragma("unroll") for (int k_ = 0; k_ < 8; ++k_) acc[k_] *= al_; \
    _Pragma("unroll") for (int t_ = 0; t_ < 4; ++t_) fma8(acc, s4_[t_], Vr[s_][t_]); \
    asm volatile("" : "+v"(acc[0]), "+v"(acc[1]), "+v"(acc[2]), "+v"(acc[3]), "+v"(acc[4]), "+v"(acc[5]), "+v"(acc[6]), "+v"(acc[7]), "+v"(l), "+v"(m) :: "memory"); } while (0)
  unsigned kx = 0;
#define STEP(s_, i_) do { if (i_ < 256u) PROC(s_); SB(); \
    i_ = kx < 25u ? 53u + 8u * kx + w : (w == 7 && kx < 28u ? 228u + kx : 256u); ++kx; ISSUE(s_, i_); SB(); } while (0)
#define ROUNDS(n_) for (int it_ = 0; it_ < (n_); ++it_) { STEP(0, i0); STEP(1, i1); }
  if (w < 7) PROC(0);
  SB(); i0 = 29 + w; ISSUE(0, i0); SB();
  if (w < 7) PROC(1);
  SB(); i1 = 37 + w; ISSUE(1, i1); SB();
  CBAR();
  WAITV(29);
  if (w < 7) { FROMLDS(3, reinterpret_cast<const unsigned*>(red) + w * 2048); PROC(3); SB(); }
  FROMLDS(3, w1s + w * 2560); PROC(3); SB();
  PROC(2); SB();
  CBAR();
  if (w < 5) mod_chunk(wm0, wm1, cv, part + 320, &ctr[1], modg_all, colB, tag, true, w, lane, c);
  asm volatile("" :: "v"(to), "v"(t1), "v"(t2) : "memory");
  {
    u32x4 t[10];
#pragma unroll
    for (int i = 0; i < 10; ++i) t[i] = BLD(rW1, (lane >> 3) * (2 * FF * 2) + (lane & 7) * 16, (w * 80 + (i >> 1) * 16 + (i & 1)) * (FF * 2) + fs * 128, 0);
    CBAR();
    ROUNDS(1);
    CBAR();
#pragma unroll
    for (int i = 0; i < 5; ++i) {
      const int pr = w * 40 + i * 8 + (lane >> 3);
#pragma unroll
      for (int k = 0; k < 4; ++k) PAIRUP(t[2 * i][k], t[2 * i + 1][k]);
      *reinterpret_cast<u32x4*>(w1s + pr * 64 + (((2 * (lane & 7)) ^ (pr & 15)) * 4)) = u32x4{t[2 * i][0], t[2 * i + 1][0], t[2 * i][1], t[2 * i + 1][1]};
      *reinterpret_cast<u32x4*>(w1s + pr * 64 + (((2 * (lane & 7) + 1) ^ (pr & 15)) * 4)) = u32x4{t[2 * i][2], t[2 * i + 1][2], t[2 * i][3], t[2 * i + 1][3]};
    }
  }
  CBAR();
  const int hf = lane >> 5, l32 = lane & 31;
  u32x2 wo[40], w2[20];
#pragma unroll
  for (int i = 0; i < 8; ++i)
#pragma unroll
    for (int jb = 0; jb < 5; ++jb) wo[i * 5 + jb] = BLD2(rWo, hf * 8 * (DM * 2) + l32 * 8, (16 * w + i) * (DM * 2) + jb * 256);
  CBAR();
  ROUNDS(1);
  CBAR();
#pragma unroll
  for (int i = 0; i < 4; ++i)
#pragma unroll
    for (int jb = 0; jb < 5; ++jb) w2[i * 5 + jb] = BLD2(rW2, hf * 4 * (DM * 2) + l32 * 8, (8 * w + i) * (DM * 2) + jb * 256);
  CBAR();
#pragma unroll
  for (int i = 0; i < 8; i += 2)
#pragma unroll
    for (int jb = 0; jb < 5; ++jb) { PAIRUP(wo[i * 5 + jb][0], wo[i * 5 + 5 + jb][0]); PAIRUP(wo[i * 5 + jb][1], wo[i * 5 + 5 + jb][1]);
      asm volatile("" : "+v"(wo[i * 5 + jb]), "+v"(wo[i * 5 + 5 + jb])); }
  ROUNDS(2);
  CBAR();
#pragma unroll
  for (int i = 0; i < 4; i += 2)
#pragma unroll
    for (int jb = 0; jb < 5; ++jb) { PAIRUP(w2[i * 5 + jb][0], w2[i * 5 + 5 + jb][0]); PAIRUP(w2[i * 5 + jb][1], w2[i * 5 + 5 + jb][1]);
      asm volatile("" : "+v"(w2[i * 5 + jb]), "+v"(w2[i * 5 + 5 + jb])); }
  {
    u32x4 g[3];
    for (unsigned spins = 0;; ++spins) {
#pragma unroll
      for (int j = 0; j < 3; ++j) g[j] = BLD(rs, (j < 2 || tid < 256 ? tid : 0) * 16, go + 2 * DM * 8 + j * NT * 16, 16);
      CBAR();
      ROUNDS(1);
      bool ok = true;
#pragma unroll
      for (int j = 0; j < 3; ++j) ok &= (g[j][0] >> 16) == tlo && g[j][1] == thi && (g[j][2] >> 16) == tlo && g[j][3] == thi;
      if (__all(ok) || spins > SPIN_CAP) break;
    }
#pragma unroll
    for (int j = 0; j < 3; ++j) if (j < 2 || tid < 256) {
      const int k = 2 * (tid + NT * j);
      const float add = (k >= 2 * DM && k < 3 * DM) ? 1.f : 0.f;
      *reinterpret_cast<f32x2*>(modr + k) = f32x2{bflo(g[j][0]) + add, bflo(g[j][2]) + add};
    }
  }
  CBAR();
  do { STEP(0, i0); STEP(1, i1); } while (i0 < 256u);
  CBAR();
  TS(4);
  {
    const float M = xrow_max(m), f = __builtin_amdgcn_exp2f(m - M);
    l = xrow_sum(l * f);
#pragma unroll
    for (int k = 0; k < 8; ++k) acc[k] *= f;
    float o0, o1; fold8(acc, o0, o1);
    *reinterpret_cast<f32x2*>(mrg + w * (DH + 4) + 4 + 8 * c + 2 * r) = f32x2{o0, o1};
    if (lane == 0) *reinterpret_cast<f32x2*>(mrg + w * (DH + 4)) = f32x2{M, l};
  }
  BAR();
  TSW(10, 0);
  {
    f32x2 ml[NW]; float av[NW];
#pragma unroll
    for (int u = 0; u < NW; ++u) { ml[u] = *reinterpret_cast<const f32x2*>(mrg + u * (DH + 4)); av[u] = mrg[u * (DH + 4) + 4 + 16 * w + c]; }
    float M = ml[0][0];
#pragma unroll
    for (int u = 1; u < NW; ++u) M = fmaxf(M, ml[u][0]);
    float L = 0.f, ov = 0.f;
#pragma unroll
    for (int u = 0; u < NW; ++u) { const float f = __builtin_amdgcn_exp2f(ml[u][0] - M); L = fmaf(f, ml[u][1], L); ov = fmaf(f, av[u], ov); }
    if (lane < 16) ob[16 * w + c] = (u16)cvtpk(ov * __builtin_amdgcn_rcpf(L), 0.f);
    LDSWAIT(); CBAR();
    const u32x4 op = *reinterpret_cast<const u32x4*>(ob + 16 * w + 8 * hf);
#pragma unroll
    for (int jb = 0; jb < 5; ++jb) {
      float a[4] = {0.f, 0.f, 0.f, 0.f};
#pragma unroll
      for (int p = 0; p < 4; ++p) {
        a[0] = dot2(wo[p * 10 + jb][0], op[p], a[0]); a[1] = dot2(wo[p * 10 + 5 + jb][0], op[p], a[1]);
        a[2] = dot2(wo[p * 10 + jb][1], op[p], a[2]); a[3] = dot2(wo[p * 10 + 5 + jb][1], op[p], a[3]);
      }
      *reinterpret_cast<f32x2*>(red + w * DM + jb * 128 + 4 * l32 + 2 * hf) = f32x2{fold32(a[0], a[2]), fold32(a[1], a[3])};
    }
  }
  BAR();
  if (tid < DM / 4) {
    f32x4 s = *reinterpret_cast<const f32x4*>(red + tid * 4);
#pragma unroll
    for (int u = 1; u < NW; ++u) s += *reinterpret_cast<const f32x4*>(red + u * DM + tid * 4);
    const u32x4 o = {nosent(s[0]), nosent(s[1]), nosent(s[2]), nosent(s[3])};
    __builtin_amdgcn_raw_buffer_store_b128(o, rs, p1off + h * DM * 4 + tid * 16, 0, 16);
  }
  CBAR();
  TS(5);
  float* red2 = red + NW * DM;
  f32x2 sb[5], cb[5];
  u32x4 wa[5], wb[5];
#pragma unroll
  for (int i = 0; i < 5; ++i) {
    sb[i] = *reinterpret_cast<const f32x2*>(modr + DM + 2 * lane + 128 * i); cb[i] = *reinterpret_cast<const f32x2*>(modr + 2 * DM + 2 * lane + 128 * i);
    wa[i] = *reinterpret_cast<const u32x4*>(w1s + (lane + 64 * i) * 64 + (((2 * w) ^ c) * 4));
    wb[i] = *reinterpret_cast<const u32x4*>(w1s + (lane + 64 * i) * 64 + (((2 * w + 1) ^ c) * 4));
  }
  const f32x2 ga = *reinterpret_cast<const f32x2*>(modr + (tid < DM / 2 ? tid * 2 : 0));
  CBAR();
  {
    f32x4 s[3];
    const int c2 = lane + 128 < DM / 4 ? lane + 128 : lane;
    for (unsigned spins = 0;; ++spins) {
      u32x4 g[12];
#pragma unroll
      for (int p = 0; p < 4; ++p) {
        const unsigned base = p1off + (4 * w + p) * DM * 4;
        g[p * 3 + 0] = BLD(rs, lane * 16, base, 16);
        g[p * 3 + 1] = BLD(rs, lane * 16, base + 1024, 16);
        g[p * 3 + 2] = BLD(rs, c2 * 16, base, 16);
      }
      unsigned mn = 0;
#pragma unroll
      for (int k = 0; k < 12; ++k) mn = max(max(mn, max(g[k][0], g[k][1])), max(g[k][2], g[k][3]));
#pragma unroll
      for (int k = 0; k < 3; ++k)
        s[k] = (__builtin_bit_cast(f32x4, g[k]) + __builtin_bit_cast(f32x4, g[3 + k])) + (__builtin_bit_cast(f32x4, g[6 + k]) + __builtin_bit_cast(f32x4, g[9 + k]));
      if (__all(mn != SENT) || spins > SPIN_CAP) break;
      __builtin_amdgcn_s_sleep(2);
      CBAR();
    }
    *reinterpret_cast<f32x4*>(red2 + w * DM + lane * 4) = s[0];
    *reinterpret_cast<f32x4*>(red2 + w * DM + (lane + 64) * 4) = s[1];
    if (lane < 32) *reinterpret_cast<f32x4*>(red2 + w * DM + (lane + 128) * 4) = s[2];
  }
  TS(6);
  BAR();
  if (tid < DM / 2) {
    f32x2 s = *reinterpret_cast<const f32x2*>(red2 + tid * 2);
#pragma unroll
    for (int u = 1; u < NW; ++u) s += *reinterpret_cast<const f32x2*>(red2 + u * DM + tid * 2);
    const unsigned sb2 = cvtpk(s[0], s[1]);
    const unsigned pr2 = cvtpk(ga[0] * bflo(sb2), ga[1] * bfhi(sb2));
    const unsigned x2 = cvtpk(bflo(xa) + bflo(pr2), bfhi(xa) + bfhi(pr2));
    *reinterpret_cast<f32x2*>(vec + tid * 2) = f32x2{bflo(x2), bfhi(x2)};
  }
  BAR();
  if (h < NFW) {
    f32x2 x1[5];
#pragma unroll
    for (int i = 0; i < 5; ++i) x1[i] = *reinterpret_cast<const f32x2*>(vec + 2 * lane + 128 * i);
    float mu = 0.f;
#pragma unroll
    for (int i = 0; i < 5; ++i) mu += x1[i][0] + x1[i][1];
    mu = wave_sum(mu) * (1.f / DM);
    float vr = 0.f;
#pragma unroll
    for (int i = 0; i < 5; ++i) { x1[i] -= mu; vr = fmaf(x1[i][0], x1[i][0], fmaf(x1[i][1], x1[i][1], vr)); }
    vr = wave_sum(vr) * (1.f / DM);
    const float rs2 = __builtin_amdgcn_rsqf(vr + 1e-5f);
    float a[8] = {0.f, 0.f, 0.f, 0.f, 0.f, 0.f, 0.f, 0.f};
#pragma unroll
    for (int i = 0; i < 5; ++i) {
      const unsigned h2 = cvtpk(fmaf(x1[i][0] * rs2, cb[i][0], sb[i][0]), fmaf(x1[i][1] * rs2, cb[i][1], sb[i][1]));
#pragma unroll
      for (int k = 0; k < 4; ++k) { a[k] = dot2(wa[i][k], h2, a[k]); a[4 + k] = dot2(wb[i][k], h2, a[4 + k]); }
    }
    float o0, o1; fold8(a, o0, o1); o0 = silu(rbf(row_sum(o0))); o1 = silu(rbf(row_sum(o1)));
    const unsigned pk = cvtpk(o0, o1);
    auto ab = __builtin_amdgcn_permlane16_swap(pk, pk, false, false);
#pragma unroll
    for (int jb = 0; jb < 5; ++jb) {
      float a2[4] = {0.f, 0.f, 0.f, 0.f};
#pragma unroll
      for (int p = 0; p < 2; ++p) {
        a2[0] = dot2(w2[p * 10 + jb][0], ab[p], a2[0]); a2[1] = dot2(w2[p * 10 + 5 + jb][0], ab[p], a2[1]);
        a2[2] = dot2(w2[p * 10 + jb][1], ab[p], a2[2]); a2[3] = dot2(w2[p * 10 + 5 + jb][1], ab[p], a2[3]);
      }
      *reinterpret_cast<f32x2*>(red + w * DM + jb * 128 + 4 * l32 + 2 * hf) = f32x2{fold32(a2[0], a2[2]), fold32(a2[1], a2[3])};
    }
    BAR();
    if (tid < DM / 4) {
      f32x4 s = *reinterpret_cast<const f32x4*>(red + tid * 4);
#pragma unroll
      for (int u = 1; u < NW; ++u) s += *reinterpret_cast<const f32x4*>(red + u * DM + tid * 4);
      const u32x4 o = {nosent(s[0]), nosent(s[1]), nosent(s[2]), nosent(s[3])};
      __builtin_amdgcn_raw_buffer_store_b128(o, rs, p2off + h * DM * 4 + tid * 16, 0, 16);
    }
    CBAR();
  }
  TS(7);
  if (w == 3) {
    const int ch = lane % 5, pg = lane / 5;
    f32x4 s;
    for (unsigned spins = 0;; ++spins) {
      u32x4 g[3];
      bool ok = true;
      s = f32x4{0.f, 0.f, 0.f, 0.f};
#pragma unroll
      for (int k = 0; k < 3; ++k) {
        const int p = pg + 12 * k; const bool live = lane < 60 && p < NFW;
        g[k] = __builtin_amdgcn_raw_buffer_load_b128(rs, p2off + (live ? p : 0) * DM * 4 + (h * 5 + ch) * 16, 0, 16);
      }
#pragma unroll
      for (int k = 0; k < 3; ++k) {
        const int p = pg + 12 * k; const bool live = lane < 60 && p < NFW;
        ok &= g[k][0] != SENT && g[k][1] != SENT && g[k][2] != SENT && g[k][3] != SENT;
        if (live) s += __builtin_bit_cast(f32x4, g[k]);
      }
      if (__all(ok) || spins > SPIN_CAP) break;
      __builtin_amdgcn_s_sleep(2);
      CBAR();
    }
    *reinterpret_cast<f32x4*>(mrg + lane * 4) = s;
    __builtin_amdgcn_s_waitcnt(0xC07F);
    CBAR();
    if (lane < 20) {
      float t = 0.f;
#pragma unroll
      for (int u = 0; u < 12; ++u) t += mrg[u * 20 + lane];
      const int col = h * 20 + lane;
      const float o = vec[col] + rbf(modr[3 * DM + col] * rbf(t));
      Out[b * DM + col] = (u16)(cvtpk(o, 0.f) & 0xffffu);
    }
    if (bid == 0 && lane == 0) stg(reinterpret_cast<u64*>(ws + WS_EPOCH), epoch + 1);
    TSW(8, 3); TSR(17, 3);
  }
}

extern "C" void kernel_launch(void* const* d_in, const int* in_sizes, int n_in, void* d_out, int out_size, void* d_ws, size_t ws_size,
                              hipStream_t stream) {
  (void)in_sizes; (void)n_in; (void)out_size; (void)ws_size;
  hipLaunchKernelGGL(fused_block, dim3(NB * NH), dim3(NT), 0, stream, (const u16*)d_in[0], (const u16*)d_in[1], (const u16*)d_in[2],
                     (const u16*)d_in[3], (const u16*)d_in[4], (const u16*)d_in[6], (const u16*)d_in[7], (const u16*)d_in[8],
                     (const u16*)d_in[9], (u16*)d_out, (unsigned char*)d_ws);
}
```
